```python
import torch

def _softmax(x, axis=-1):
    return torch.softmax(x, dim=axis)
def _gelu(x):
    return torch.nn.functional.gelu(x, approximate="tanh")
def _silu(x):
    return torch.nn.functional.silu(x)
def _sigmoid(x):
    return torch.sigmoid(x)
def _layernorm(x, axis=-1, eps=1e-5):
    m = x.mean(dim=axis, keepdim=True)
    v = x.var(dim=axis, keepdim=True, unbiased=False)
    return (x - m) / torch.sqrt(v + eps)
def _scatter_add(tgt, idx, src, axis=0):
    return tgt.clone().index_add_(axis, idx, src)
def _segment_sum(src, seg, n):
    out = torch.zeros((n,) + src.shape[1:], dtype=src.dtype)
    return out.index_add_(0, seg, src)
def _pad(x, pw):
    flat = tuple(v for pair in reversed(pw) for v in pair)
    return torch.nn.functional.pad(x, flat)

kernel_name = 'concat_heads__sliding_block_cached__gqa_block_cached__decode'

def setup_inputs():

    _w = (lambda a: (lambda t: t.to(torch.bfloat16) if t.is_floating_point() else t)(torch.as_tensor(a)))
    def _A_setup():
        B, Sq, D = 8, 1, 512
        Hq, Hkv, Sk, Dh = 32, 8, 4096, 128
        assert Hq % Hkv == 0, (Hq, Hkv)
        return {
            "x": _w((lambda *s: torch.randn(*s, dtype=torch.bfloat16) * 0.8)(B, Sq, D)),
            "k_cache": _w((lambda *s: torch.randn(*s, dtype=torch.bfloat16) * 1.6)(B, Hkv, Sk, Dh)),
            "v_cache": _w((lambda *s: torch.randn(*s, dtype=torch.bfloat16) * 0.2)(B, Hkv, Sk, Dh)),
            "wq": _w((lambda *s: torch.randn(*s, dtype=torch.bfloat16) * 1.6)(D, Hq * Dh) * (D ** -0.5)),
            "wo": _w((lambda *s: torch.randn(*s, dtype=torch.bfloat16) * 0.2)(Hq * Dh, D) * ((Hq * Dh) ** -0.5)),
        }
    _A_in = _A_setup()

    def _B_setup():
        B, Sq, D = 8, 1, 512
        Hq, Hkv, Sk, Dh = 32, 8, 4096, 128
        assert Hq % Hkv == 0, (Hq, Hkv)
        return {
            "x": _w((lambda *s: torch.randn(*s, dtype=torch.bfloat16) * 0.8)(B, Sq, D)),
            "k_cache": _w((lambda *s: torch.randn(*s, dtype=torch.bfloat16) * 1.6)(B, Hkv, Sk, Dh)),
            "v_cache": _w((lambda *s: torch.randn(*s, dtype=torch.bfloat16) * 0.2)(B, Hkv, Sk, Dh)),
            "wq": _w((lambda *s: torch.randn(*s, dtype=torch.bfloat16) * 1.6)(D, Hq * Dh) * (D ** -0.5)),
            "wo": _w((lambda *s: torch.randn(*s, dtype=torch.bfloat16) * 0.2)(Hq * Dh, D) * ((Hq * Dh) ** -0.5)),
        }
    _B_in = _B_setup()

    return {
        "A_x": _A_in["x"],
        "A_k_cache": _A_in["k_cache"],
        "A_v_cache": _A_in["v_cache"],
        "A_wq": _A_in["wq"],
        "A_wo": _A_in["wo"],
        "B_x": _B_in["x"],
        "B_k_cache": _B_in["k_cache"],
        "B_v_cache": _B_in["v_cache"],
        "B_wq": _B_in["wq"],
        "B_wo": _B_in["wo"],
    }

def reference(A_x, A_k_cache, A_v_cache, A_wq, A_wo, B_x, B_k_cache, B_v_cache, B_wq, B_wo):
    def _A_ref(x, k_cache, v_cache, wq, wo):
        B, Sq, D = x.shape
        Hq, Dh = 32, 128
        Hkv, Sk = k_cache.shape[1], k_cache.shape[2]
        W = min(4096, max(256, Sk // 4))
        rep = Hq // Hkv
        q = (lambda x, *d: x.permute(*d))((x @ wq).reshape(B, Sq, Hq, Dh), 0, 2, 1, 3)
        k = (lambda x, n, axis: torch.repeat_interleave(x, n, dim=axis))(k_cache[:, :, Sk - W:, :], rep, axis=1)
        v = (lambda x, n, axis: torch.repeat_interleave(x, n, dim=axis))(v_cache[:, :, Sk - W:, :], rep, axis=1)
        scores = torch.einsum("bhqd,bhkd->bhqk", q, k) * (Dh ** -0.5)
        p = _softmax(scores, axis=-1)
        o = torch.einsum("bhqk,bhkd->bhqd", p, v)
        return (lambda x, *d: x.permute(*d))(o, 0, 2, 1, 3).reshape(B, Sq, Hq * Dh) @ wo

    def _B_ref(x, k_cache, v_cache, wq, wo):
        B, Sq, D = x.shape
        Hq, Dh = 32, 128
        Hkv = k_cache.shape[1]
        rep = Hq // Hkv
        q = (lambda x, *d: x.permute(*d))((x @ wq).reshape(B, Sq, Hq, Dh), 0, 2, 1, 3)
        k = (lambda x, n, axis: torch.repeat_interleave(x, n, dim=axis))(k_cache, rep, axis=1)
        v = (lambda x, n, axis: torch.repeat_interleave(x, n, dim=axis))(v_cache, rep, axis=1)
        scores = torch.einsum("bhqd,bhkd->bhqk", q, k) * (Dh ** -0.5)
        p = _softmax(scores, axis=-1)
        o = torch.einsum("bhqk,bhkd->bhqd", p, v)
        return (lambda x, *d: x.permute(*d))(o, 0, 2, 1, 3).reshape(B, Sq, Hq * Dh) @ wo

    a = _A_ref(A_x, A_k_cache, A_v_cache, A_wq, A_wo)
    b = _B_ref(B_x, B_k_cache, B_v_cache, B_wq, B_wo)
    return torch.concatenate([a, b], axis=-1)
```

```cpp
#include <hip/hip_runtime.h>
#include <stdint.h>

#define GAS __attribute__((address_space(1)))
#define LAS __attribute__((address_space(3)))
typedef unsigned v4u __attribute__((ext_vector_type(4)));
typedef unsigned v2u __attribute__((ext_vector_type(2)));
typedef float f32x4 __attribute__((ext_vector_type(4)));
typedef __bf16 bf16x8 __attribute__((ext_vector_type(8)));
typedef __bf16 bf16x2 __attribute__((ext_vector_type(2)));
typedef short s16x4 __attribute__((ext_vector_type(4)));
typedef GAS unsigned long long gu64;
typedef GAS unsigned gu32;

namespace {

constexpr int SK = 4096, DH = 128;
constexpr float SCL = 0.08838834764831845f * 1.4426950408889634f;
constexpr unsigned kSpinCap = 1u << 14;

constexpr int EPOCH_OFF = 0;
constexpr int Q_OFF = 4096, Q_BYTES = 2 * 8 * 2048 * 8;
constexpr int P_OFF = Q_OFF + Q_BYTES, P_BYTES = 2 * 8 * 4 * 8 * 4 * 64 * 8;
constexpr int L_OFF = P_OFF + P_BYTES, L_BYTES = 2 * 8 * 4 * 8 * 4 * 8;
constexpr int Y_OFF = L_OFF + L_BYTES, Y_BYTES = 2 * 8 * 8 * 4 * 32 * 16 * 8;
constexpr int WS_BYTES = Y_OFF + Y_BYTES;

constexpr int LDS_P1 = 0;
constexpr int LDS_Q = 8192;
constexpr int QROW = 272, QBR = 4 * QROW;
constexpr int LDS_ML = LDS_Q + 2 * QBR + 128;
constexpr int LDS_MRG = LDS_ML + 256;
constexpr int MROW = 132;
constexpr int LDS_WO = LDS_MRG + 2 * 4 * 4 * MROW * 4;
constexpr int LDS_OB = LDS_WO + 32768;
constexpr int LDS_RED = LDS_OB + 2 * 8 * 272;
constexpr int LDS_END = LDS_RED + 512;
constexpr int LDS_BYTES = 84 * 1024;
static_assert(LDS_END <= LDS_BYTES, "lds");
static_assert(LDS_ML % 16 == 0 && LDS_MRG % 16 == 0 && LDS_WO % 16 == 0 && LDS_OB % 16 == 0 && LDS_RED % 16 == 0, "align");

struct Params {
  const char* xA; const char* kA; const char* vA; const char* wqA; const char* woA;
  const char* xB; const char* kB; const char* vB; const char* wqB; const char* woB;
  char* out; char* ws;
};

template <typename T, typename F> __device__ __forceinline__ T bc(F x) { return __builtin_bit_cast(T, x); }
__device__ __forceinline__ f32x4 mfma(v4u a, v4u b, f32x4 c) {
  return __builtin_amdgcn_mfma_f32_16x16x32_bf16(bc<bf16x8>(a), bc<bf16x8>(b), c, 0, 0, 0);
}
__device__ __forceinline__ unsigned pk(float lo, float hi) { bf16x2 r = {(__bf16)lo, (__bf16)hi}; return bc<unsigned>(r); }
__device__ __forceinline__ float blo(unsigned u) { return bc<float>(u << 16); }
__device__ __forceinline__ float bhi(unsigned u) { return bc<float>(u & 0xffff0000u); }
__device__ __forceinline__ v4u ldnt(const GAS char* p) { return __builtin_nontemporal_load((const GAS v4u*)p); }
__device__ __forceinline__ v4u ldsc(__amdgpu_buffer_rsrc_t rs, int off) { return __builtin_amdgcn_raw_buffer_load_b128(rs, off, 0, 16); }
__device__ __forceinline__ void st_granule(char* ws, int off, unsigned val, unsigned tag) {
  __hip_atomic_store((gu64*)(ws + off), ((unsigned long long)tag << 32) | val, __ATOMIC_RELAXED, __HIP_MEMORY_SCOPE_AGENT);
}
#define SB() __builtin_amdgcn_sched_barrier(0)
#define WAITVM(n) asm volatile("s_waitcnt vmcnt(" #n ")" ::: "memory")
#define RAW_BARRIER() asm volatile("s_waitcnt lgkmcnt(0)\n\ts_barrier" ::: "memory")

__device__ __forceinline__ v4u tcol(const v4u (&r)[8], int d) {
  const unsigned sel = (d & 1) ? 0x07060302u : 0x05040100u;
  const int c = d >> 1;
  v4u f;
  f.x = __builtin_amdgcn_perm(r[1][c], r[0][c], sel);
  f.y = __builtin_amdgcn_perm(r[3][c], r[2][c], sel);
  f.z = __builtin_amdgcn_perm(r[5][c], r[4][c], sel);
  f.w = __builtin_amdgcn_perm(r[7][c], r[6][c], sel);
  return f;
}
__device__ __forceinline__ float xrow_max(float x) {
  auto s = __builtin_amdgcn_permlane16_swap(bc<unsigned>(x), bc<unsigned>(x), false, false);
  x = fmaxf(bc<float>(s[0]), bc<float>(s[1]));
  auto t = __builtin_amdgcn_permlane32_swap(bc<unsigned>(x), bc<unsigned>(x), false, false);
  return fmaxf(bc<float>(t[0]), bc<float>(t[1]));
}
__device__ __forceinline__ float xrow_sum(float x) {
  auto s = __builtin_amdgcn_permlane16_swap(bc<unsigned>(x), bc<unsigned>(x), false, false);
  x = bc<float>(s[0]) + bc<float>(s[1]);
  auto t = __builtin_amdgcn_permlane32_swap(bc<unsigned>(x), bc<unsigned>(x), false, false);
  return bc<float>(t[0]) + bc<float>(t[1]);
}
template <int CTRL> __device__ __forceinline__ float dpp(float x) {
  return bc<float>(__builtin_amdgcn_mov_dpp(bc<int>(x), CTRL, 0xf, 0xf, true));
}
constexpr int DPP_ROR8 = 0x128;

struct LaneOff { unsigned k0, k1, v0, v1; };
__device__ __forceinline__ v4u ldK(const GAS char* t, const LaneOff& o, int j) { return ldnt(t + ((j < 4) ? o.k0 : o.k1) + (j & 3) * 64); }
__device__ __forceinline__ v4u ldV(const GAS char* t, const LaneOff& o, int j) { return ldnt(t + ((j < 4) ? o.v0 : o.v1) + (j & 3) * 1024); }

template <bool NEXT>
__device__ __forceinline__ void do_tile(v4u (&K)[8], v4u (&V)[8], const v4u (&qf)[4], f32x4 (&acc)[8], float& m, float& l,
                                        const GAS char* nk, const GAS char* nv, const LaneOff& lo) {
  f32x4 s0 = {0.f, 0.f, 0.f, 0.f}, s1 = {0.f, 0.f, 0.f, 0.f};
#pragma unroll
  for (int st = 0; st < 4; ++st) { s0 = mfma(K[st], qf[st], s0); s1 = mfma(K[4 + st], qf[st], s1); }
  SB();
  if (NEXT) {
#pragma unroll
    for (int j = 0; j < 8; ++j) K[j] = ldK(nk, lo, j);
  }
  SB();
  float t = fmaxf(fmaxf(fmaxf(s0[0], s0[1]), fmaxf(s0[2], s0[3])), fmaxf(fmaxf(s1[0], s1[1]), fmaxf(s1[2], s1[3])));
  t = xrow_max(t);
  const float mn = fmaxf(m, t);
  const float alpha = __builtin_amdgcn_exp2f((m - mn) * SCL);
  const float mc = mn * SCL;
  float p[8];
#pragma unroll
  for (int r = 0; r < 4; ++r) {
    p[r] = __builtin_amdgcn_exp2f(s0[r] * SCL - mc);
    p[4 + r] = __builtin_amdgcn_exp2f(s1[r] * SCL - mc);
  }
  l = l * alpha + (((p[0] + p[1]) + (p[2] + p[3])) + ((p[4] + p[5]) + (p[6] + p[7])));
  m = mn;
  const v4u P = {pk(p[0], p[1]), pk(p[2], p[3]), pk(p[4], p[5]), pk(p[6], p[7])};
#pragma unroll
  for (int d = 0; d < 8; ++d) {
    acc[d] *= alpha;
    acc[d] = mfma(tcol(V, d), P, acc[d]);
  }
  SB();
  if (NEXT) {
#pragma unroll
    for (int j = 0; j < 8; ++j) V[j] = ldV(nv, lo, j);
  }
  SB();
}

__device__ __forceinline__ void park_partial(LAS char* L, int br, int w, int lane, const f32x4 (&acc)[8], float m, float l) {
  const float ls = xrow_sum(l);
  const int n = lane & 15, g = lane >> 4;
  if (n < 4) {
    LAS float* o = (LAS float*)(L + LDS_MRG) + ((br * 4 + w) * 4 + n) * MROW + g * 32;
#pragma unroll
    for (int d = 0; d < 8; ++d) *(LAS f32x4*)(o + d * 4) = acc[d];
    if (g == 0) { LAS float* ml = (LAS float*)(L + LDS_ML) + ((br * 4 + w) * 4 + n) * 2; ml[0] = m; ml[1] = ls; }
  }
}

__global__ void __launch_bounds__(256, 2) fused_decode(Params p) {
  extern __shared__ __attribute__((aligned(16))) char smem[];
  LAS char* L = (LAS char*)smem;
  const int tid = threadIdx.x, lane = tid & 63, w = __builtin_amdgcn_readfirstlane(tid >> 6);
  const int wg = blockIdx.x, kvh = wg & 7, jj = wg >> 3;
  const __amdgpu_buffer_rsrc_t rs = __builtin_amdgcn_make_buffer_rsrc(p.ws, 0, WS_BYTES, 0x00020000);

  const unsigned tag = __hip_atomic_load((gu32*)(p.ws + EPOCH_OFF), __ATOMIC_RELAXED, __HIP_MEMORY_SCOPE_AGENT) + 1u;
  const int br1 = jj >> 4, cg = jj & 15;
  v4u xa[2], wv[8];
  {
    const GAS char* x = (const GAS char*)(br1 ? p.xB : p.xA);
    const int m = lane & 15, g = lane >> 4, h1 = m >> 3, b = m & 7;
#pragma unroll
    for (int ps = 0; ps < 2; ++ps) xa[ps] = *(const GAS v4u*)(x + (b * 512 + 128 * w + 8 * (4 * g + 2 * ps + h1)) * 2);
    const GAS char* wq = (const GAS char*)(br1 ? p.wqB : p.wqA) + (size_t)(128 * w) * 8192 + (kvh * 512 + 32 * cg) * 2;
    const unsigned wo_ = (unsigned)(8 * (lane >> 2)) * 8192u + (lane & 3) * 16u;
#pragma unroll
    for (int j = 0; j < 8; ++j) wv[j] = ldnt(wq + wo_ + j * 8192u);
  }
  SB();

  const int b2 = jj >> 2, s2 = jj & 3;
  const size_t pair = (size_t)(b2 * 8 + kvh) * SK * 256;
  LaneOff lo;
  {
    const int i = lane & 15, g = lane >> 4;
    lo.k0 = (unsigned)(4 * (i & 3) + (i >> 2)) * 256u + g * 16u;
    lo.k1 = lo.k0 + 16u * 256u;
    lo.v0 = (unsigned)lane * 16u;
    lo.v1 = lo.v0 + 4096u;
  }
  const GAS char* kA = (const GAS char*)p.kA + pair + (size_t)(3072 + 256 * s2 + 32 * w) * 256;
  const GAS char* vA = (const GAS char*)p.vA + pair + (size_t)(3072 + 256 * s2 + 32 * w) * 256;
  const GAS char* kB = (const GAS char*)p.kB + pair + (size_t)(1024 * s2 + 32 * w) * 256;
  const GAS char* vB = (const GAS char*)p.vB + pair + (size_t)(1024 * s2 + 32 * w) * 256;
  constexpr int TS = 128 * 256;

  v4u K0[8], V0[8], K1[8], V1[8];
#pragma unroll
  for (int j = 0; j < 8; ++j) K0[j] = ldK(kA, lo, j);
  SB();

  {
    f32x4 a0[8], a1[8];
#pragma unroll
    for (int d = 0; d < 8; ++d) {
      const v4u f = tcol(wv, d);
      const f32x4 z = {0.f, 0.f, 0.f, 0.f};
      a0[d] = mfma(xa[0], f, z);
      a1[d] = mfma(xa[1], f, z);
    }
    const int n = lane & 15, g = lane >> 4, kk = n >> 2, c2 = n & 3, h1 = g >> 1;
    const bool hi = (kk >> 1) != 0;
    LAS float* dst = (LAS float*)(L + LDS_P1) + ((w * 2 + h1) * 2 + (g & 1)) * 128 + c2 * 32;
#pragma unroll
    for (int d = 0; d < 8; ++d) {
      f32x4 v;
#pragma unroll
      for (int r = 0; r < 4; ++r) { const float t = hi ? a1[d][r] : a0[d][r]; v[r] = t + dpp<DPP_ROR8>(t); }
      if (kk == h1) *(LAS f32x4*)(dst + d * 4) = v;
    }
  }
  RAW_BARRIER();
  if (tid < 128) {
    const int b = tid >> 4, pr = tid & 15;
    float q0 = 0.f, q1 = 0.f;
    const int c0 = 2 * pr;
    const LAS float* src = (const LAS float*)(L + LDS_P1) + (b >> 2) * 128 + (c0 >> 3) * 32 + (c0 & 7) * 4 + (b & 3);
#pragma unroll
    for (int u = 0; u < 8; ++u) { q0 += src[u * 256]; q1 += src[u * 256 + 4]; }
    st_granule(p.ws, Q_OFF + ((br1 * 8 + b) * 2048 + kvh * 256 + cg * 16 + pr) * 8, pk(q0, q1), tag);
  }
  SB();

#pragma unroll
  for (int j = 0; j < 8; ++j) { WAITVM(6); V0[j] = ldV(vA, lo, j); SB(); }
#pragma unroll
  for (int j = 0; j < 8; ++j) { WAITVM(6); K1[j] = ldK(kA + TS, lo, j); SB(); }
  const int qoff = Q_OFF + (((w >> 1) * 8 + b2) * 2048 + kvh * 256 + (w & 1) * 128 + 2 * lane) * 8;
  WAITVM(6);
  v4u qg = ldsc(rs, qoff);
  SB();
#pragma unroll
  for (int j = 0; j < 8; ++j) { WAITVM(6); V1[j] = ldV(vA + TS, lo, j); SB(); }
  {
    if (!__all(qg.y == tag && qg.w == tag)) {
      unsigned spins = 0;
      do {
        __builtin_amdgcn_s_sleep(2);
        asm volatile("" ::: "memory");
        qg = ldsc(rs, qoff);
      } while (!__all(qg.y == tag && qg.w == tag) && ++spins < kSpinCap);
    }
    const int col = (w & 1) * 256 + 4 * lane;
    *(LAS v2u*)(L + LDS_Q + (w >> 1) * QBR + (col >> 7) * QROW + (col & 127) * 2) = v2u{qg.x, qg.z};
  }
  RAW_BARRIER();

  v4u qf[4];
  f32x4 acc[8];
  float m, l;
  const int qlane = (lane & 3) * QROW + (lane >> 4) * 16;
#pragma unroll
  for (int st = 0; st < 4; ++st) qf[st] = *(const LAS v4u*)(L + LDS_Q + qlane + st * 64);
#pragma unroll
  for (int d = 0; d < 8; ++d) acc[d] = f32x4{0.f, 0.f, 0.f, 0.f};
  m = -INFINITY; l = 0.f;
  do_tile<true>(K0, V0, qf, acc, m, l, kB, vB, lo);
  do_tile<true>(K1, V1, qf, acc, m, l, kB + TS, vB + TS, lo);
  park_partial(L, 0, w, lane, acc, m, l);
#pragma unroll
  for (int st = 0; st < 4; ++st) qf[st] = *(const LAS v4u*)(L + LDS_Q + QBR + qlane + st * 64);
#pragma unroll
  for (int d = 0; d < 8; ++d) acc[d] = f32x4{0.f, 0.f, 0.f, 0.f};
  m = -INFINITY; l = 0.f;
  do_tile<true>(K0, V0, qf, acc, m, l, kB + 2 * TS, vB + 2 * TS, lo);
  do_tile<true>(K1, V1, qf, acc, m, l, kB + 3 * TS, vB + 3 * TS, lo);
  do_tile<true>(K0, V0, qf, acc, m, l, kB + 4 * TS, vB + 4 * TS, lo);
  do_tile<true>(K1, V1, qf, acc, m, l, kB + 5 * TS, vB + 5 * TS, lo);
  do_tile<true>(K0, V0, qf, acc, m, l, kB + 6 * TS, vB + 6 * TS, lo);
  do_tile<true>(K1, V1, qf, acc, m, l, kB + 7 * TS, vB + 7 * TS, lo);
  do_tile<false>(K0, V0, qf, acc, m, l, nullptr, nullptr, lo);
  const int hh = jj >> 3, cs = jj & 7;
  {
    const size_t wbase = (size_t)((kvh * 4 + hh) * 128 + 32 * w) * 1024 + cs * 128;
    const unsigned wl = (unsigned)(lane >> 3) * 1024u + (lane & 7) * 16u;
    const GAS char* wa = (const GAS char*)p.woA + wbase;
    const GAS char* wb = (const GAS char*)p.woB + wbase;
#pragma unroll
    for (int i = 0; i < 4; ++i) { K0[i] = ldnt(wa + wl + i * 8192u); K0[4 + i] = ldnt(wb + wl + i * 8192u); }
  }
  SB();
  do_tile<false>(K1, V1, qf, acc, m, l, nullptr, nullptr, lo);
  park_partial(L, 1, w, lane, acc, m, l);
  {
#pragma unroll
    for (int i = 0; i < 4; ++i) {
      *(LAS v4u*)(L + LDS_WO + (32 * w + 8 * i) * 128 + lane * 16) = K0[i];
      *(LAS v4u*)(L + LDS_WO + 16384 + (32 * w + 8 * i) * 128 + lane * 16) = K0[4 + i];
    }
  }
  RAW_BARRIER();

  {
    const int h = tid >> 6, dp = tid & 63, D = 2 * dp;
    const int idx = (D >> 5) * 32 + (D & 7) * 4 + ((D >> 3) & 3);
#pragma unroll
    for (int br = 0; br < 2; ++br) {
      const LAS float* ml = (const LAS float*)(L + LDS_ML) + (br * 16 + h) * 2;
      float mw[4], lw[4];
#pragma unroll
      for (int u = 0; u < 4; ++u) { mw[u] = ml[u * 8]; lw[u] = ml[u * 8 + 1]; }
      const float M = fmaxf(fmaxf(mw[0], mw[1]), fmaxf(mw[2], mw[3]));
      float Ls = 0.f, o0 = 0.f, o1 = 0.f;
#pragma unroll
      for (int u = 0; u < 4; ++u) {
        const float f = __builtin_amdgcn_exp2f((mw[u] - M) * SCL);
        const LAS float* o = (const LAS float*)(L + LDS_MRG) + ((br * 4 + u) * 4 + h) * MROW + idx;
        Ls += f * lw[u]; o0 += f * o[0]; o1 += f * o[4];
      }
      const float inv = 1.0f / Ls;
      const int slot = ((br * 8 + kvh) * 4 + h) * 32 + b2 * 4 + s2;
      st_granule(p.ws, P_OFF + (slot * 64 + dp) * 8, pk(o0 * inv, o1 * inv), tag);
      if (dp == 0) st_granule(p.ws, L_OFF + slot * 8, bc<unsigned>(M * SCL + __builtin_amdgcn_logf(Ls)), tag);
    }
  }

  {
    const int b = tid >> 5, dq = tid & 31;
    v4u pa[2][4], la[2][2];
    unsigned spins = 0;
    for (;;) {
      asm volatile("" ::: "memory");
      bool ok = true;
#pragma unroll
      for (int br = 0; br < 2; ++br) {
        const int slot = ((br * 8 + kvh) * 4 + hh) * 32 + b * 4;
#pragma unroll
        for (int s = 0; s < 4; ++s) pa[br][s] = ldsc(rs, P_OFF + ((slot + s) * 64 + 2 * dq) * 8);
        la[br][0] = ldsc(rs, L_OFF + slot * 8);
        la[br][1] = ldsc(rs, L_OFF + slot * 8 + 16);
      }
#pragma unroll
      for (int br = 0; br < 2; ++br) {
#pragma unroll
        for (int s = 0; s < 4; ++s) ok = ok && pa[br][s].y == tag && pa[br][s].w == tag;
        ok = ok && la[br][0].y == tag && la[br][0].w == tag && la[br][1].y == tag && la[br][1].w == tag;
      }
      if (__all(ok)) break;
      if (++spins > kSpinCap) break;
      __builtin_amdgcn_s_sleep(8);
    }
#pragma unroll
    for (int br = 0; br < 2; ++br) {
      const float l0 = bc<float>(la[br][0].x), l1 = bc<float>(la[br][0].z), l2 = bc<float>(la[br][1].x), l3 = bc<float>(la[br][1].z);
      const float M = fmaxf(fmaxf(l0, l1), fmaxf(l2, l3));
      const float ws_[4] = {__builtin_amdgcn_exp2f(l0 - M), __builtin_amdgcn_exp2f(l1 - M), __builtin_amdgcn_exp2f(l2 - M), __builtin_amdgcn_exp2f(l3 - M)};
      const float inv = 1.0f / ((ws_[0] + ws_[1]) + (ws_[2] + ws_[3]));
      float o[4] = {0.f, 0.f, 0.f, 0.f};
#pragma unroll
      for (int s = 0; s < 4; ++s) {
        o[0] += ws_[s] * blo(pa[br][s].x); o[1] += ws_[s] * bhi(pa[br][s].x);
        o[2] += ws_[s] * blo(pa[br][s].z); o[3] += ws_[s] * bhi(pa[br][s].z);
      }
      *(LAS v2u*)(L + LDS_OB + (br * 8 + b) * 272 + dq * 8) = v2u{pk(o[0] * inv, o[1] * inv), pk(o[2] * inv, o[3] * inv)};
    }
  }
  RAW_BARRIER();
  {
    const int n = lane & 15, g = lane >> 4;
#pragma unroll
    for (int br = 0; br < 2; ++br) {
      f32x4 y = {0.f, 0.f, 0.f, 0.f};
#pragma unroll
      for (int st = 0; st < 4; ++st) {
        const v4u a = *(const LAS v4u*)(L + LDS_OB + (br * 8 + (n & 7)) * 272 + st * 64 + g * 16);
        const int row = 32 * st + 8 * g + (n >> 2);
        const LAS char* wp = L + LDS_WO + br * 16384 + row * 128 + w * 32 + (n & 3) * 8;
        const s16x4 b0 = __builtin_amdgcn_ds_read_tr16_b64_v4i16((LAS s16x4*)wp);
        const s16x4 b1 = __builtin_amdgcn_ds_read_tr16_b64_v4i16((LAS s16x4*)(wp + 512));
        const v2u u0 = bc<v2u>(b0), u1 = bc<v2u>(b1);
        y = mfma(a, v4u{u0.x, u0.y, u1.x, u1.y}, y);
      }
      if (g < 2) {
#pragma unroll
        for (int r = 0; r < 4; ++r) {
          const int b = 4 * g + r;
          st_granule(p.ws, Y_OFF + (((((br * 8 + cs) * 8 + b) * 4 + w) * 32 + kvh * 4 + hh) * 16 + n) * 8, bc<unsigned>(y[r]), tag);
        }
      }
    }
  }

  {
    const int pr = tid >> 3, np = tid & 7;
    v4u ya[2];
    unsigned spins = 0;
    for (;;) {
      asm volatile("" ::: "memory");
#pragma unroll
      for (int br = 0; br < 2; ++br) ya[br] = ldsc(rs, Y_OFF + (((((br * 8 + cs) * 8 + kvh) * 4 + hh) * 32 + pr) * 16 + 2 * np) * 8);
      const bool ok = ya[0].y == tag && ya[0].w == tag && ya[1].y == tag && ya[1].w == tag;
      if (__all(ok)) break;
      if (++spins > kSpinCap) break;
      __builtin_amdgcn_s_sleep(4);
    }
    float v[4] = {bc<float>(ya[0].x), bc<float>(ya[0].z), bc<float>(ya[1].x), bc<float>(ya[1].z)};
#pragma unroll
    for (int i = 0; i < 4; ++i) { v[i] += dpp<DPP_ROR8>(v[i]); v[i] = xrow_sum(v[i]); }
    if (lane < 8) {
      LAS float* red = (LAS float*)(L + LDS_RED) + w * 32;
      red[2 * np] = v[0]; red[2 * np + 1] = v[1]; red[16 + 2 * np] = v[2]; red[16 + 2 * np + 1] = v[3];
    }
  }
  RAW_BARRIER();
  if (tid < 16) {
    const int br = tid >> 3, np = tid & 7;
    const LAS float* red = (const LAS float*)(L + LDS_RED) + br * 16 + 2 * np;
    const float o0 = (red[0] + red[32]) + (red[64] + red[96]);
    const float o1 = (red[1] + red[33]) + (red[65] + red[97]);
    *(GAS unsigned*)((GAS char*)p.out + (kvh * 1024 + br * 512 + cs * 64 + hh * 16 + 2 * np) * 2) = pk(o0, o1);
  }
  if (wg == 0 && tid == 0) __hip_atomic_store((gu32*)(p.ws + EPOCH_OFF), tag, __ATOMIC_RELAXED, __HIP_MEMORY_SCOPE_AGENT);
}

}

extern "C" void kernel_launch(void* const* d_in, const int* in_sizes, int n_in, void* d_out, int out_size, void* d_ws,
                              size_t ws_size, hipStream_t stream) {
  (void)in_sizes; (void)n_in; (void)out_size; (void)ws_size;
  Params p;
  p.xA = (const char*)d_in[0]; p.kA = (const char*)d_in[1]; p.vA = (const char*)d_in[2]; p.wqA = (const char*)d_in[3]; p.woA = (const char*)d_in[4];
  p.xB = (const char*)d_in[5]; p.kB = (const char*)d_in[6]; p.vB = (const char*)d_in[7]; p.wqB = (const char*)d_in[8]; p.woB = (const char*)d_in[9];
  p.out = (char*)d_out; p.ws = (char*)d_ws;
  (void)hipFuncSetAttribute((const void*)fused_decode, hipFuncAttributeMaxDynamicSharedMemorySize, LDS_BYTES);
  hipLaunchKernelGGL(fused_decode, dim3(256), dim3(256), LDS_BYTES, stream, p);
}
```

```cpp
#define PROBE 0
#include <hip/hip_runtime.h>
#include <stdint.h>

#define GAS __attribute__((address_space(1)))
#define LAS __attribute__((address_space(3)))
typedef unsigned v4u __attribute__((ext_vector_type(4)));
typedef unsigned v2u __attribute__((ext_vector_type(2)));
typedef float f32x4 __attribute__((ext_vector_type(4)));
typedef __bf16 bf16x8 __attribute__((ext_vector_type(8)));
typedef __bf16 bf16x2 __attribute__((ext_vector_type(2)));
typedef short s16x4 __attribute__((ext_vector_type(4)));
typedef GAS unsigned long long gu64;
typedef GAS unsigned gu32;

#define STAMP(i) do { if ((i) == PROBE) { __builtin_amdgcn_sched_barrier(0); st_ = (unsigned)__builtin_amdgcn_s_memrealtime(); __builtin_amdgcn_sched_barrier(0); } } while (0)
#define STAMP_DECL unsigned st_ = 0
#define STAMP_FLUSH() do { while ((unsigned)__builtin_amdgcn_s_memrealtime() - st_ < 4000u) __builtin_amdgcn_s_sleep(1); } while (0)

namespace {

constexpr int SK = 4096, DH = 128;
constexpr float SCL = 0.08838834764831845f * 1.4426950408889634f;
constexpr unsigned kSpinCap = 1u << 14;

constexpr int EPOCH_OFF = 0;
constexpr int Q_OFF = 4096, Q_BYTES = 2 * 8 * 2048 * 8;
constexpr int P_OFF = Q_OFF + Q_BYTES, P_BYTES = 2 * 8 * 4 * 8 * 4 * 64 * 8;
constexpr int L_OFF = P_OFF + P_BYTES, L_BYTES = 2 * 8 * 4 * 8 * 4 * 8;
constexpr int Y_OFF = L_OFF + L_BYTES, Y_BYTES = 2 * 8 * 8 * 4 * 32 * 16 * 8;
constexpr int WS_BYTES = Y_OFF + Y_BYTES;

constexpr int LDS_P1 = 0;
constexpr int LDS_Q = 8192;
constexpr int QROW = 272, QBR = 4 * QROW;
constexpr int LDS_ML = LDS_Q + 2 * QBR + 128;
constexpr int LDS_MRG = LDS_ML + 256;
constexpr int MROW = 132;
constexpr int LDS_WO = LDS_MRG + 2 * 4 * 4 * MROW * 4;
constexpr int LDS_OB = LDS_WO + 32768;
constexpr int LDS_RED = LDS_OB + 2 * 8 * 272;
constexpr int LDS_END = LDS_RED + 512;
constexpr int LDS_BYTES = 84 * 1024;
static_assert(LDS_END <= LDS_BYTES, "lds");
static_assert(LDS_ML % 16 == 0 && LDS_MRG % 16 == 0 && LDS_WO % 16 == 0 && LDS_OB % 16 == 0 && LDS_RED % 16 == 0, "align");

struct Params {
  const char* xA; const char* kA; const char* vA; const char* wqA; const char* woA;
  const char* xB; const char* kB; const char* vB; const char* wqB; const char* woB;
  char* out; char* ws;
};

template <typename T, typename F> __device__ __forceinline__ T bc(F x) { return __builtin_bit_cast(T, x); }
__device__ __forceinline__ f32x4 mfma(v4u a, v4u b, f32x4 c) {
  return __builtin_amdgcn_mfma_f32_16x16x32_bf16(bc<bf16x8>(a), bc<bf16x8>(b), c, 0, 0, 0);
}
__device__ __forceinline__ unsigned pk(float lo, float hi) { bf16x2 r = {(__bf16)lo, (__bf16)hi}; return bc<unsigned>(r); }
__device__ __forceinline__ float blo(unsigned u) { return bc<float>(u << 16); }
__device__ __forceinline__ float bhi(unsigned u) { return bc<float>(u & 0xffff0000u); }
__device__ __forceinline__ v4u ldnt(const GAS char* p) { return __builtin_nontemporal_load((const GAS v4u*)p); }
__device__ __forceinline__ v4u ldsc(__amdgpu_buffer_rsrc_t rs, int off) { return __builtin_amdgcn_raw_buffer_load_b128(rs, off, 0, 16); }
__device__ __forceinline__ void st_granule(char* ws, int off, unsigned val, unsigned tag) {
  __hip_atomic_store((gu64*)(ws + off), ((unsigned long long)tag << 32) | val, __ATOMIC_RELAXED, __HIP_MEMORY_SCOPE_AGENT);
}
#define SB() __builtin_amdgcn_sched_barrier(0)
#define WAITVM(n) asm volatile("s_waitcnt vmcnt(" #n ")" ::: "memory")
#define RAW_BARRIER() asm volatile("s_waitcnt lgkmcnt(0)\n\ts_barrier" ::: "memory")

__device__ __forceinline__ v4u tcol(const v4u (&r)[8], int d) {
  const unsigned sel = (d & 1) ? 0x07060302u : 0x05040100u;
  const int c = d >> 1;
  v4u f;
  f.x = __builtin_amdgcn_perm(r[1][c], r[0][c], sel);
  f.y = __builtin_amdgcn_perm(r[3][c], r[2][c], sel);
  f.z = __builtin_amdgcn_perm(r[5][c], r[4][c], sel);
  f.w = __builtin_amdgcn_perm(r[7][c], r[6][c], sel);
  return f;
}
__device__ __forceinline__ float xrow_max(float x) {
  auto s = __builtin_amdgcn_permlane16_swap(bc<unsigned>(x), bc<unsigned>(x), false, false);
  x = fmaxf(bc<float>(s[0]), bc<float>(s[1]));
  auto t = __builtin_amdgcn_permlane32_swap(bc<unsigned>(x), bc<unsigned>(x), false, false);
  return fmaxf(bc<float>(t[0]), bc<float>(t[1]));
}
__device__ __forceinline__ float xrow_sum(float x) {
  auto s = __builtin_amdgcn_permlane16_swap(bc<unsigned>(x), bc<unsigned>(x), false, false);
  x = bc<float>(s[0]) + bc<float>(s[1]);
  auto t = __builtin_amdgcn_permlane32_swap(bc<unsigned>(x), bc<unsigned>(x), false, false);
  return bc<float>(t[0]) + bc<float>(t[1]);
}
template <int CTRL> __device__ __forceinline__ float dpp(float x) {
  return bc<float>(__builtin_amdgcn_mov_dpp(bc<int>(x), CTRL, 0xf, 0xf, true));
}
constexpr int DPP_ROR8 = 0x128;

struct LaneOff { unsigned k0, k1, v0, v1; };
__device__ __forceinline__ v4u ldK(const GAS char* t, const LaneOff& o, int j) { return ldnt(t + ((j < 4) ? o.k0 : o.k1) + (j & 3) * 64); }
__device__ __forceinline__ v4u ldV(const GAS char* t, const LaneOff& o, int j) { return ldnt(t + ((j < 4) ? o.v0 : o.v1) + (j & 3) * 1024); }

template <bool NEXT>
__device__ __forceinline__ void do_tile(v4u (&K)[8], v4u (&V)[8], const v4u (&qf)[4], f32x4 (&acc)[8], float& m, float& l,
                                        const GAS char* nk, const GAS char* nv, const LaneOff& lo) {
  f32x4 s0 = {0.f, 0.f, 0.f, 0.f}, s1 = {0.f, 0.f, 0.f, 0.f};
#pragma unroll
  for (int st = 0; st < 4; ++st) { s0 = mfma(K[st], qf[st], s0); s1 = mfma(K[4 + st], qf[st], s1); }
  SB();
  if (NEXT) {
#pragma unroll
    for (int j = 0; j < 8; ++j) K[j] = ldK(nk, lo, j);
  }
  SB();
  float t = fmaxf(fmaxf(fmaxf(s0[0], s0[1]), fmaxf(s0[2], s0[3])), fmaxf(fmaxf(s1[0], s1[1]), fmaxf(s1[2], s1[3])));
  t = xrow_max(t);
  const float mn = fmaxf(m, t);
  const float alpha = __builtin_amdgcn_exp2f((m - mn) * SCL);
  const float mc = mn * SCL;
  float p[8];
#pragma unroll
  for (int r = 0; r < 4; ++r) {
    p[r] = __builtin_amdgcn_exp2f(s0[r] * SCL - mc);
    p[4 + r] = __builtin_amdgcn_exp2f(s1[r] * SCL - mc);
  }
  l = l * alpha + (((p[0] + p[1]) + (p[2] + p[3])) + ((p[4] + p[5]) + (p[6] + p[7])));
  m = mn;
  const v4u P = {pk(p[0], p[1]), pk(p[2], p[3]), pk(p[4], p[5]), pk(p[6], p[7])};
#pragma unroll
  for (int d = 0; d < 8; ++d) {
    acc[d] *= alpha;
    acc[d] = mfma(tcol(V, d), P, acc[d]);
  }
  SB();
  if (NEXT) {
#pragma unroll
    for (int j = 0; j < 8; ++j) V[j] = ldV(nv, lo, j);
  }
  SB();
}

__device__ __forceinline__ void park_partial(LAS char* L, int br, int w, int lane, const f32x4 (&acc)[8], float m, float l) {
  const float ls = xrow_sum(l);
  const int n = lane & 15, g = lane >> 4;
  if (n < 4) {
    LAS float* o = (LAS float*)(L + LDS_MRG) + ((br * 4 + w) * 4 + n) * MROW + g * 32;
#pragma unroll
    for (int d = 0; d < 8; ++d) *(LAS f32x4*)(o + d * 4) = acc[d];
    if (g == 0) { LAS float* ml = (LAS float*)(L + LDS_ML) + ((br * 4 + w) * 4 + n) * 2; ml[0] = m; ml[1] = ls; }
  }
}

__global__ void __launch_bounds__(256, 2) fused_decode(Params p) {
  extern __shared__ __attribute__((aligned(16))) char smem[];
  LAS char* L = (LAS char*)smem;
  const int tid = threadIdx.x, lane = tid & 63, w = __builtin_amdgcn_readfirstlane(tid >> 6);
  const int wg = blockIdx.x, kvh = wg & 7, jj = wg >> 3;
  const __amdgpu_buffer_rsrc_t rs = __builtin_amdgcn_make_buffer_rsrc(p.ws, 0, WS_BYTES, 0x00020000);

  STAMP_DECL; STAMP(0);
  const unsigned tag = __hip_atomic_load((gu32*)(p.ws + EPOCH_OFF), __ATOMIC_RELAXED, __HIP_MEMORY_SCOPE_AGENT) + 1u;
  const int br1 = jj >> 4, cg = jj & 15;
  v4u xa[2], wv[8];
  {
    const GAS char* x = (const GAS char*)(br1 ? p.xB : p.xA);
    const int m = lane & 15, g = lane >> 4, h1 = m >> 3, b = m & 7;
#pragma unroll
    for (int ps = 0; ps < 2; ++ps) xa[ps] = *(const GAS v4u*)(x + (b * 512 + 128 * w + 8 * (4 * g + 2 * ps + h1)) * 2);
    const GAS char* wq = (const GAS char*)(br1 ? p.wqB : p.wqA) + (size_t)(128 * w) * 8192 + (kvh * 512 + 32 * cg) * 2;
    const unsigned wo_ = (unsigned)(8 * (lane >> 2)) * 8192u + (lane & 3) * 16u;
#pragma unroll
    for (int j = 0; j < 8; ++j) wv[j] = ldnt(wq + wo_ + j * 8192u);
  }
  SB();

  const int b2 = jj >> 2, s2 = jj & 3;
  const size_t pair = (size_t)(b2 * 8 + kvh) * SK * 256;
  LaneOff lo;
  {
    const int i = lane & 15, g = lane >> 4;
    lo.k0 = (unsigned)(4 * (i & 3) + (i >> 2)) * 256u + g * 16u;
    lo.k1 = lo.k0 + 16u * 256u;
    lo.v0 = (unsigned)lane * 16u;
    lo.v1 = lo.v0 + 4096u;
  }
  const GAS char* kA = (const GAS char*)p.kA + pair + (size_t)(3072 + 256 * s2 + 32 * w) * 256;
  const GAS char* vA = (const GAS char*)p.vA + pair + (size_t)(3072 + 256 * s2 + 32 * w) * 256;
  const GAS char* kB = (const GAS char*)p.kB + pair + (size_t)(1024 * s2 + 32 * w) * 256;
  const GAS char* vB = (const GAS char*)p.vB + pair + (size_t)(1024 * s2 + 32 * w) * 256;
  constexpr int TS = 128 * 256;

  v4u K0[8], V0[8], K1[8], V1[8];
#pragma unroll
  for (int j = 0; j < 8; ++j) K0[j] = ldK(kA, lo, j);
  SB();

  {
    f32x4 a0[8], a1[8];
#pragma unroll
    for (int d = 0; d < 8; ++d) {
      const v4u f = tcol(wv, d);
      const f32x4 z = {0.f, 0.f, 0.f, 0.f};
      a0[d] = mfma(xa[0], f, z);
      a1[d] = mfma(xa[1], f, z);
    }
    const int n = lane & 15, g = lane >> 4, kk = n >> 2, c2 = n & 3, h1 = g >> 1;
    const bool hi = (kk >> 1) != 0;
    LAS float* dst = (LAS float*)(L + LDS_P1) + ((w * 2 + h1) * 2 + (g & 1)) * 128 + c2 * 32;
#pragma unroll
    for (int d = 0; d < 8; ++d) {
      f32x4 v;
#pragma unroll
      for (int r = 0; r < 4; ++r) { const float t = hi ? a1[d][r] : a0[d][r]; v[r] = t + dpp<DPP_ROR8>(t); }
      if (kk == h1) *(LAS f32x4*)(dst + d * 4) = v;
    }
  }
  STAMP(1);
  RAW_BARRIER();
  if (tid < 128) {
    const int b = tid >> 4, pr = tid & 15;
    float q0 = 0.f, q1 = 0.f;
    const int c0 = 2 * pr;
    const LAS float* src = (const LAS float*)(L + LDS_P1) + (b >> 2) * 128 + (c0 >> 3) * 32 + (c0 & 7) * 4 + (b & 3);
#pragma unroll
    for (int u = 0; u < 8; ++u) { q0 += src[u * 256]; q1 += src[u * 256 + 4]; }
    st_granule(p.ws, Q_OFF + ((br1 * 8 + b) * 2048 + kvh * 256 + cg * 16 + pr) * 8, pk(q0, q1), tag);
  }
  STAMP(2);
  SB();

#pragma unroll
  for (int j = 0; j < 8; ++j) { WAITVM(6); V0[j] = ldV(vA, lo, j); SB(); }
#pragma unroll
  for (int j = 0; j < 8; ++j) { WAITVM(6); K1[j] = ldK(kA + TS, lo, j); SB(); }
  const int qoff = Q_OFF + (((w >> 1) * 8 + b2) * 2048 + kvh * 256 + (w & 1) * 128 + 2 * lane) * 8;
  WAITVM(6);
  v4u qg = ldsc(rs, qoff);
  SB();
#pragma unroll
  for (int j = 0; j < 8; ++j) { WAITVM(6); V1[j] = ldV(vA + TS, lo, j); SB(); }
  STAMP(3);
  {
    if (!__all(qg.y == tag && qg.w == tag)) {
      unsigned spins = 0;
      do {
        __builtin_amdgcn_s_sleep(2);
        asm volatile("" ::: "memory");
        qg = ldsc(rs, qoff);
      } while (!__all(qg.y == tag && qg.w == tag) && ++spins < kSpinCap);
    }
    const int col = (w & 1) * 256 + 4 * lane;
    *(LAS v2u*)(L + LDS_Q + (w >> 1) * QBR + (col >> 7) * QROW + (col & 127) * 2) = v2u{qg.x, qg.z};
  }
  STAMP(4);
  RAW_BARRIER();
  STAMP(5);

  v4u qf[4];
  f32x4 acc[8];
  float m, l;
  const int qlane = (lane & 3) * QROW + (lane >> 4) * 16;
#pragma unroll
  for (int st = 0; st < 4; ++st) qf[st] = *(const LAS v4u*)(L + LDS_Q + qlane + st * 64);
#pragma unroll
  for (int d = 0; d < 8; ++d) acc[d] = f32x4{0.f, 0.f, 0.f, 0.f};
  m = -INFINITY; l = 0.f;
  do_tile<true>(K0, V0, qf, acc, m, l, kB, vB, lo);
  do_tile<true>(K1, V1, qf, acc, m, l, kB + TS, vB + TS, lo);
  park_partial(L, 0, w, lane, acc, m, l);
  STAMP(6);
#pragma unroll
  for (int st = 0; st < 4; ++st) qf[st] = *(const LAS v4u*)(L + LDS_Q + QBR + qlane + st * 64);
#pragma unroll
  for (int d = 0; d < 8; ++d) acc[d] = f32x4{0.f, 0.f, 0.f, 0.f};
  m = -INFINITY; l = 0.f;
  do_tile<true>(K0, V0, qf, acc, m, l, kB + 2 * TS, vB + 2 * TS, lo);
  do_tile<true>(K1, V1, qf, acc, m, l, kB + 3 * TS, vB + 3 * TS, lo);
  do_tile<true>(K0, V0, qf, acc, m, l, kB + 4 * TS, vB + 4 * TS, lo);
  do_tile<true>(K1, V1, qf, acc, m, l, kB + 5 * TS, vB + 5 * TS, lo);
  do_tile<true>(K0, V0, qf, acc, m, l, kB + 6 * TS, vB + 6 * TS, lo);
  do_tile<true>(K1, V1, qf, acc, m, l, kB + 7 * TS, vB + 7 * TS, lo);
  do_tile<false>(K0, V0, qf, acc, m, l, nullptr, nullptr, lo);
  const int hh = jj >> 3, cs = jj & 7;
  {
    const size_t wbase = (size_t)((kvh * 4 + hh) * 128 + 32 * w) * 1024 + cs * 128;
    const unsigned wl = (unsigned)(lane >> 3) * 1024u + (lane & 7) * 16u;
    const GAS char* wa = (const GAS char*)p.woA + wbase;
    const GAS char* wb = (const GAS char*)p.woB + wbase;
#pragma unroll
    for (int i = 0; i < 4; ++i) { K0[i] = ldnt(wa + wl + i * 8192u); K0[4 + i] = ldnt(wb + wl + i * 8192u); }
  }
  SB();
  do_tile<false>(K1, V1, qf, acc, m, l, nullptr, nullptr, lo);
  park_partial(L, 1, w, lane, acc, m, l);
  {
#pragma unroll
    for (int i = 0; i < 4; ++i) {
      *(LAS v4u*)(L + LDS_WO + (32 * w + 8 * i) * 128 + lane * 16) = K0[i];
      *(LAS v4u*)(L + LDS_WO + 16384 + (32 * w + 8 * i) * 128 + lane * 16) = K0[4 + i];
    }
  }
  STAMP(7);
  RAW_BARRIER();
  STAMP(8);

  {
    const int h = tid >> 6, dp = tid & 63, D = 2 * dp;
    const int idx = (D >> 5) * 32 + (D & 7) * 4 + ((D >> 3) & 3);
#pragma unroll
    for (int br = 0; br < 2; ++br) {
      const LAS float* ml = (const LAS float*)(L + LDS_ML) + (br * 16 + h) * 2;
      float mw[4], lw[4];
#pragma unroll
      for (int u = 0; u < 4; ++u) { mw[u] = ml[u * 8]; lw[u] = ml[u * 8 + 1]; }
      const float M = fmaxf(fmaxf(mw[0], mw[1]), fmaxf(mw[2], mw[3]));
      float Ls = 0.f, o0 = 0.f, o1 = 0.f;
#pragma unroll
      for (int u = 0; u < 4; ++u) {
        const float f = __builtin_amdgcn_exp2f((mw[u] - M) * SCL);
        const LAS float* o = (const LAS float*)(L + LDS_MRG) + ((br * 4 + u) * 4 + h) * MROW + idx;
        Ls += f * lw[u]; o0 += f * o[0]; o1 += f * o[4];
      }
      const float inv = 1.0f / Ls;
      const int slot = ((br * 8 + kvh) * 4 + h) * 32 + b2 * 4 + s2;
      st_granule(p.ws, P_OFF + (slot * 64 + dp) * 8, pk(o0 * inv, o1 * inv), tag);
      if (dp == 0) st_granule(p.ws, L_OFF + slot * 8, bc<unsigned>(M * SCL + __builtin_amdgcn_logf(Ls)), tag);
    }
  }

  STAMP(9);
  {
    const int b = tid >> 5, dq = tid & 31;
    v4u pa[2][4], la[2][2];
    unsigned spins = 0;
    for (;;) {
      asm volatile("" ::: "memory");
      bool ok = true;
#pragma unroll
      for (int br = 0; br < 2; ++br) {
        const int slot = ((br * 8 + kvh) * 4 + hh) * 32 + b * 4;
#pragma unroll
        for (int s = 0; s < 4; ++s) pa[br][s] = ldsc(rs, P_OFF + ((slot + s) * 64 + 2 * dq) * 8);
        la[br][0] = ldsc(rs, L_OFF + slot * 8);
        la[br][1] = ldsc(rs, L_OFF + slot * 8 + 16);
      }
#pragma unroll
      for (int br = 0; br < 2; ++br) {
#pragma unroll
        for (int s = 0; s < 4; ++s) ok = ok && pa[br][s].y == tag && pa[br][s].w == tag;
        ok = ok && la[br][0].y == tag && la[br][0].w == tag && la[br][1].y == tag && la[br][1].w == tag;
      }
      if (__all(ok)) break;
      if (++spins > kSpinCap) break;
      __builtin_amdgcn_s_sleep(8);
    }
    STAMP(10);
#pragma unroll
    for (int br = 0; br < 2; ++br) {
      const float l0 = bc<float>(la[br][0].x), l1 = bc<float>(la[br][0].z), l2 = bc<float>(la[br][1].x), l3 = bc<float>(la[br][1].z);
      const float M = fmaxf(fmaxf(l0, l1), fmaxf(l2, l3));
      const float ws_[4] = {__builtin_amdgcn_exp2f(l0 - M), __builtin_amdgcn_exp2f(l1 - M), __builtin_amdgcn_exp2f(l2 - M), __builtin_amdgcn_exp2f(l3 - M)};
      const float inv = 1.0f / ((ws_[0] + ws_[1]) + (ws_[2] + ws_[3]));
      float o[4] = {0.f, 0.f, 0.f, 0.f};
#pragma unroll
      for (int s = 0; s < 4; ++s) {
        o[0] += ws_[s] * blo(pa[br][s].x); o[1] += ws_[s] * bhi(pa[br][s].x);
        o[2] += ws_[s] * blo(pa[br][s].z); o[3] += ws_[s] * bhi(pa[br][s].z);
      }
      *(LAS v2u*)(L + LDS_OB + (br * 8 + b) * 272 + dq * 8) = v2u{pk(o[0] * inv, o[1] * inv), pk(o[2] * inv, o[3] * inv)};
    }
  }
  RAW_BARRIER();
  {
    const int n = lane & 15, g = lane >> 4;
#pragma unroll
    for (int br = 0; br < 2; ++br) {
      f32x4 y = {0.f, 0.f, 0.f, 0.f};
#pragma unroll
      for (int st = 0; st < 4; ++st) {
        const v4u a = *(const LAS v4u*)(L + LDS_OB + (br * 8 + (n & 7)) * 272 + st * 64 + g * 16);
        const int row = 32 * st + 8 * g + (n >> 2);
        const LAS char* wp = L + LDS_WO + br * 16384 + row * 128 + w * 32 + (n & 3) * 8;
        const s16x4 b0 = __builtin_amdgcn_ds_read_tr16_b64_v4i16((LAS s16x4*)wp);
        const s16x4 b1 = __builtin_amdgcn_ds_read_tr16_b64_v4i16((LAS s16x4*)(wp + 512));
        const v2u u0 = bc<v2u>(b0), u1 = bc<v2u>(b1);
        y = mfma(a, v4u{u0.x, u0.y, u1.x, u1.y}, y);
      }
      if (g < 2) {
#pragma unroll
        for (int r = 0; r < 4; ++r) {
          const int b = 4 * g + r;
          st_granule(p.ws, Y_OFF + (((((br * 8 + cs) * 8 + b) * 4 + w) * 32 + kvh * 4 + hh) * 16 + n) * 8, bc<unsigned>(y[r]), tag);
        }
      }
    }
  }

  STAMP(11);
  {
    const int pr = tid >> 3, np = tid & 7;
    v4u ya[2];
    unsigned spins = 0;
    for (;;) {
      asm volatile("" ::: "memory");
#pragma unroll
      for (int br = 0; br < 2; ++br) ya[br] = ldsc(rs, Y_OFF + (((((br * 8 + cs) * 8 + kvh) * 4 + hh) * 32 + pr) * 16 + 2 * np) * 8);
      const bool ok = ya[0].y == tag && ya[0].w == tag && ya[1].y == tag && ya[1].w == tag;
      if (__all(ok)) break;
      if (++spins > kSpinCap) break;
      __builtin_amdgcn_s_sleep(4);
    }
    STAMP(12);
    float v[4] = {bc<float>(ya[0].x), bc<float>(ya[0].z), bc<float>(ya[1].x), bc<float>(ya[1].z)};
#pragma unroll
    for (int i = 0; i < 4; ++i) { v[i] += dpp<DPP_ROR8>(v[i]); v[i] = xrow_sum(v[i]); }
    if (lane < 8) {
      LAS float* red = (LAS float*)(L + LDS_RED) + w * 32;
      red[2 * np] = v[0]; red[2 * np + 1] = v[1]; red[16 + 2 * np] = v[2]; red[16 + 2 * np + 1] = v[3];
    }
  }
  RAW_BARRIER();
  if (tid < 16) {
    const int br = tid >> 3, np = tid & 7;
    const LAS float* red = (const LAS float*)(L + LDS_RED) + br * 16 + 2 * np;
    const float o0 = (red[0] + red[32]) + (red[64] + red[96]);
    const float o1 = (red[1] + red[33]) + (red[65] + red[97]);
    *(GAS unsigned*)((GAS char*)p.out + (kvh * 1024 + br * 512 + cs * 64 + hh * 16 + 2 * np) * 2) = pk(o0, o1);
  }
  STAMP(13);
  STAMP_FLUSH();
  if (wg == 0 && tid == 0) __hip_atomic_store((gu32*)(p.ws + EPOCH_OFF), tag, __ATOMIC_RELAXED, __HIP_MEMORY_SCOPE_AGENT);
}

}

extern "C" void kernel_launch(void* const* d_in, const int* in_sizes, int n_in, void* d_out, int out_size, void* d_ws,
                              size_t ws_size, hipStream_t stream) {
  (void)in_sizes; (void)n_in; (void)out_size; (void)ws_size;
  Params p;
  p.xA = (const char*)d_in[0]; p.kA = (const char*)d_in[1]; p.vA = (const char*)d_in[2]; p.wqA = (const char*)d_in[3]; p.woA = (const char*)d_in[4];
  p.xB = (const char*)d_in[5]; p.kB = (const char*)d_in[6]; p.vB = (const char*)d_in[7]; p.wqB = (const char*)d_in[8]; p.woB = (const char*)d_in[9];
  p.out = (char*)d_out; p.ws = (char*)d_ws;
  (void)hipFuncSetAttribute((const void*)fused_decode, hipFuncAttributeMaxDynamicSharedMemorySize, LDS_BYTES);
  hipLaunchKernelGGL(fused_decode, dim3(256), dim3(256), LDS_BYTES, stream, p);
}
```

```cpp
#include <hip/hip_runtime.h>
#include <stdint.h>
#define ILV 0
#define PACE 6

#define GAS __attribute__((address_space(1)))
#define LAS __attribute__((address_space(3)))
typedef unsigned v4u __attribute__((ext_vector_type(4)));
typedef unsigned v2u __attribute__((ext_vector_type(2)));
typedef float f32x4 __attribute__((ext_vector_type(4)));
typedef __bf16 bf16x8 __attribute__((ext_vector_type(8)));
typedef __bf16 bf16x2 __attribute__((ext_vector_type(2)));
typedef short s16x4 __attribute__((ext_vector_type(4)));
typedef GAS unsigned long long gu64;
typedef GAS unsigned gu32;

namespace {

constexpr int SK = 4096, DH = 128;
constexpr float SCL = 0.08838834764831845f * 1.4426950408889634f;
constexpr unsigned kSpinCap = 1u << 14;

constexpr int EPOCH_OFF = 0;
constexpr int Q_OFF = 4096, Q_BYTES = 2 * 8 * 2048 * 8;
constexpr int P_OFF = Q_OFF + Q_BYTES, P_BYTES = 2 * 8 * 4 * 8 * 4 * 64 * 8;
constexpr int L_OFF = P_OFF + P_BYTES, L_BYTES = 2 * 8 * 4 * 8 * 4 * 8;
constexpr int Y_OFF = L_OFF + L_BYTES, Y_BYTES = 2 * 32 * 8 * 32 * 16 * 8;
constexpr int WS_BYTES = Y_OFF + Y_BYTES;

constexpr int LDS_P1 = 0;
constexpr int LDS_Q = 8192;
constexpr int QROW = 272, QBR = 4 * QROW;
constexpr int LDS_ML = LDS_Q + 2 * QBR + 128;
constexpr int LDS_MRG = LDS_ML + 256;
constexpr int MROW = 132;
constexpr int LDS_WO = LDS_MRG + 2 * 4 * 4 * MROW * 4;
constexpr int LDS_OB = LDS_WO + 32768;
constexpr int LDS_RED = LDS_OB + 8 * 272;
constexpr int LDS_CNT = LDS_RED;
constexpr int LDS_END = LDS_CNT + 16;
constexpr int LDS_BYTES = 84 * 1024;
static_assert(LDS_END <= LDS_BYTES, "lds");
static_assert(LDS_ML % 16 == 0 && LDS_MRG % 16 == 0 && LDS_WO % 16 == 0 && LDS_OB % 16 == 0 && LDS_RED % 16 == 0, "align");

struct Params {
  const char* xA; const char* kA; const char* vA; const char* wqA; const char* woA;
  const char* xB; const char* kB; const char* vB; const char* wqB; const char* woB;
  char* out; char* ws;
};

template <typename T, typename F> __device__ __forceinline__ T bc(F x) { return __builtin_bit_cast(T, x); }
__device__ __forceinline__ f32x4 mfma(v4u a, v4u b, f32x4 c) {
  return __builtin_amdgcn_mfma_f32_16x16x32_bf16(bc<bf16x8>(a), bc<bf16x8>(b), c, 0, 0, 0);
}
__device__ __forceinline__ unsigned pk(float lo, float hi) { bf16x2 r = {(__bf16)lo, (__bf16)hi}; return bc<unsigned>(r); }
__device__ __forceinline__ float blo(unsigned u) { return bc<float>(u << 16); }
__device__ __forceinline__ float bhi(unsigned u) { return bc<float>(u & 0xffff0000u); }
__device__ __forceinline__ v4u ldnt(const GAS char* p) { return __builtin_nontemporal_load((const GAS v4u*)p); }
__device__ __forceinline__ v4u ldsc(__amdgpu_buffer_rsrc_t rs, int off) { return __builtin_amdgcn_raw_buffer_load_b128(rs, off, 0, 16); }
__device__ __forceinline__ void st_granule(char* ws, int off, unsigned val, unsigned tag) {
  __hip_atomic_store((gu64*)(ws + off), ((unsigned long long)tag << 32) | val, __ATOMIC_RELAXED, __HIP_MEMORY_SCOPE_AGENT);
}
#define SB() __builtin_amdgcn_sched_barrier(0)
#define WAITVM_(n) asm volatile("s_waitcnt vmcnt(" #n ")" ::: "memory")
#define WAITVM(n) WAITVM_(n)
#define RAW_BARRIER() asm volatile("s_waitcnt lgkmcnt(0)\n\ts_barrier" ::: "memory")

__device__ __forceinline__ v4u tcol(const v4u (&r)[8], int d) {
  const unsigned sel = (d & 1) ? 0x07060302u : 0x05040100u;
  const int c = d >> 1;
  v4u f;
  f.x = __builtin_amdgcn_perm(r[1][c], r[0][c], sel);
  f.y = __builtin_amdgcn_perm(r[3][c], r[2][c], sel);
  f.z = __builtin_amdgcn_perm(r[5][c], r[4][c], sel);
  f.w = __builtin_amdgcn_perm(r[7][c], r[6][c], sel);
  return f;
}
__device__ __forceinline__ float xrow_max(float x) {
  auto s = __builtin_amdgcn_permlane16_swap(bc<unsigned>(x), bc<unsigned>(x), false, false);
  x = fmaxf(bc<float>(s[0]), bc<float>(s[1]));
  auto t = __builtin_amdgcn_permlane32_swap(bc<unsigned>(x), bc<unsigned>(x), false, false);
  return fmaxf(bc<float>(t[0]), bc<float>(t[1]));
}
__device__ __forceinline__ float xrow_sum(float x) {
  auto s = __builtin_amdgcn_permlane16_swap(bc<unsigned>(x), bc<unsigned>(x), false, false);
  x = bc<float>(s[0]) + bc<float>(s[1]);
  auto t = __builtin_amdgcn_permlane32_swap(bc<unsigned>(x), bc<unsigned>(x), false, false);
  return bc<float>(t[0]) + bc<float>(t[1]);
}
template <int CTRL> __device__ __forceinline__ float dpp(float x) {
  return bc<float>(__builtin_amdgcn_mov_dpp(bc<int>(x), CTRL, 0xf, 0xf, true));
}
constexpr int DPP_ROR8 = 0x128;

struct LaneOff { unsigned k0, k1, v0, v1; };
__device__ __forceinline__ v4u ldK(const GAS char* t, const LaneOff& o, int j) { return ldnt(t + ((j < 4) ? o.k0 : o.k1) + (j & 3) * 64); }
__device__ __forceinline__ v4u ldV(const GAS char* t, const LaneOff& o, int j) { return ldnt(t + ((j < 4) ? o.v0 : o.v1) + (j & 3) * 1024); }

__device__ __forceinline__ void qk_part(const v4u (&K)[8], const v4u (&qf)[4], f32x4& s0, f32x4& s1) {
  s0 = f32x4{0.f, 0.f, 0.f, 0.f}; s1 = s0;
#pragma unroll
  for (int st = 0; st < 4; ++st) { s0 = mfma(K[st], qf[st], s0); s1 = mfma(K[4 + st], qf[st], s1); }
  SB();
}
__device__ __forceinline__ void sv_part(const v4u (&V)[8], const f32x4& s0, const f32x4& s1, f32x4 (&acc)[8], float& m, float& l) {
  float t = fmaxf(fmaxf(fmaxf(s0[0], s0[1]), fmaxf(s0[2], s0[3])), fmaxf(fmaxf(s1[0], s1[1]), fmaxf(s1[2], s1[3])));
  t = xrow_max(t);
  const float mn = fmaxf(m, t);
  const float alpha = __builtin_amdgcn_exp2f((m - mn) * SCL);
  const float mc = mn * SCL;
  float p[8];
#pragma unroll
  for (int r = 0; r < 4; ++r) {
    p[r] = __builtin_amdgcn_exp2f(s0[r] * SCL - mc);
    p[4 + r] = __builtin_amdgcn_exp2f(s1[r] * SCL - mc);
  }
  l = l * alpha + (((p[0] + p[1]) + (p[2] + p[3])) + ((p[4] + p[5]) + (p[6] + p[7])));
  m = mn;
  const v4u P = {pk(p[0], p[1]), pk(p[2], p[3]), pk(p[4], p[5]), pk(p[6], p[7])};
#pragma unroll
  for (int d = 0; d < 8; ++d) {
    acc[d] *= alpha;
    acc[d] = mfma(tcol(V, d), P, acc[d]);
  }
  SB();
}
__device__ __forceinline__ void fillK(v4u (&K)[8], const GAS char* t, const LaneOff& o) {
#pragma unroll
  for (int j = 0; j < 8; ++j) K[j] = ldK(t, o, j);
  SB();
}
__device__ __forceinline__ void fillV(v4u (&V)[8], const GAS char* t, const LaneOff& o) {
#pragma unroll
  for (int j = 0; j < 8; ++j) V[j] = ldV(t, o, j);
  SB();
}
__device__ __forceinline__ void dma16(const GAS char* g, unsigned lds) {
  asm volatile("s_mov_b32 m0, %0\n\ts_nop 0\n\tglobal_load_lds_dwordx4 %1, off nt" :: "s"(lds), "v"(g) : "m0", "memory");
}

__device__ __forceinline__ void park_partial(LAS char* L, int br, int w, int lane, const f32x4 (&acc)[8], float m, float l) {
  const float ls = xrow_sum(l);
  const int n = lane & 15, g = lane >> 4;
  if (n < 4) {
    LAS float* o = (LAS float*)(L + LDS_MRG) + ((br * 4 + w) * 4 + n) * MROW + g * 32;
#pragma unroll
    for (int d = 0; d < 8; ++d) *(LAS f32x4*)(o + d * 4) = acc[d];
    if (g == 0) { LAS float* ml = (LAS float*)(L + LDS_ML) + ((br * 4 + w) * 4 + n) * 2; ml[0] = m; ml[1] = ls; }
  }
}

__device__ __forceinline__ void merge_publish(LAS char* L, char* ws, int br, int h, int dp, int slot, unsigned tag) {
  const int D = 2 * dp, idx = (D >> 5) * 32 + (D & 7) * 4 + ((D >> 3) & 3);
  const LAS float* ml = (const LAS float*)(L + LDS_ML) + (br * 16 + h) * 2;
  float mw[4], lw[4], a0[4], a1[4];
#pragma unroll
  for (int u = 0; u < 4; ++u) {
    const LAS float* o = (const LAS float*)(L + LDS_MRG) + ((br * 4 + u) * 4 + h) * MROW + idx;
    mw[u] = ml[u * 8]; lw[u] = ml[u * 8 + 1]; a0[u] = o[0]; a1[u] = o[4];
  }
  const float M = fmaxf(fmaxf(mw[0], mw[1]), fmaxf(mw[2], mw[3]));
  float Ls = 0.f, o0 = 0.f, o1 = 0.f;
#pragma unroll
  for (int u = 0; u < 4; ++u) {
    const float f = __builtin_amdgcn_exp2f((mw[u] - M) * SCL);
    Ls += f * lw[u]; o0 += f * a0[u]; o1 += f * a1[u];
  }
  const float inv = 1.0f / Ls;
  st_granule(ws, P_OFF + (slot * 64 + dp) * 8, pk(o0 * inv, o1 * inv), tag);
  if (dp == 0) st_granule(ws, L_OFF + slot * 8, bc<unsigned>(M * SCL + __builtin_amdgcn_logf(Ls)), tag);
}

__global__ void __launch_bounds__(256, 2) fused_decode(Params p) {
  extern __shared__ __attribute__((aligned(16))) char smem[];
  LAS char* L = (LAS char*)smem;
  const int tid = threadIdx.x, lane = tid & 63, w = __builtin_amdgcn_readfirstlane(tid >> 6);
  const int wg = blockIdx.x, kvh = wg & 7, jj = wg >> 3;
  const __amdgpu_buffer_rsrc_t rs = __builtin_amdgcn_make_buffer_rsrc(p.ws, 0, WS_BYTES, 0x00020000);

  const unsigned tag = __hip_atomic_load((gu32*)(p.ws + EPOCH_OFF), __ATOMIC_RELAXED, __HIP_MEMORY_SCOPE_AGENT) + 1u;
  const int br1 = jj >> 4, cg = jj & 15;
  const int s2 = kvh >> 1, pr2 = (((jj - 3 * s2) & 31) << 1) | (kvh & 1), b2 = pr2 >> 3, kv2 = pr2 & 7;
  const size_t pair = (size_t)(b2 * 8 + kv2) * SK * 256;
  LaneOff lo;
  {
    const int i = lane & 15, g = lane >> 4;
    lo.k0 = (unsigned)(4 * (i & 3) + (i >> 2)) * 256u + g * 16u;
    lo.k1 = lo.k0 + 16u * 256u;
    lo.v0 = (unsigned)lane * 16u;
    lo.v1 = lo.v0 + 4096u;
  }
  const int g16 = s2 * 4 + w;
  constexpr int TB = 32 * 256;
  const GAS char* kA = (const GAS char*)p.kA + pair + (size_t)(3072 * 256 + g16 * TB);
  const GAS char* vA = (const GAS char*)p.vA + pair + (size_t)(3072 * 256 + g16 * TB);
  const GAS char* kB = (const GAS char*)p.kB + pair + (size_t)(g16 * TB);
  const GAS char* vB = (const GAS char*)p.vB + pair + (size_t)(g16 * TB);

  v4u xa[2], wv[8];
  v4u K0[8], V0[8], K1[8], V1[8];
  {
    const GAS char* x = (const GAS char*)(br1 ? p.xB : p.xA);
    const int m = lane & 15, g = lane >> 4, h1 = m >> 3, b = m & 7;
#pragma unroll
    for (int ps = 0; ps < 2; ++ps) xa[ps] = *(const GAS v4u*)(x + (b * 512 + 128 * w + 8 * (4 * g + 2 * ps + h1)) * 2);
    const GAS char* wq = (const GAS char*)(br1 ? p.wqB : p.wqA) + (size_t)(128 * w) * 8192 + (kvh * 512 + 32 * cg) * 2;
    const unsigned wo_ = (unsigned)(8 * (lane >> 2)) * 8192u + (lane & 3) * 16u;
#pragma unroll
    for (int j = 0; j < 8; ++j) wv[j] = ldnt(wq + wo_ + j * 8192u);
    SB();
#pragma unroll
    for (int j = 0; j < 8; ++j) K0[j] = ldK(kA, lo, j);
  }
  SB();

  {
    f32x4 a0[8], a1[8];
#pragma unroll
    for (int d = 0; d < 8; ++d) {
      const v4u f = tcol(wv, d);
      const f32x4 z = {0.f, 0.f, 0.f, 0.f};
      a0[d] = mfma(xa[0], f, z);
      a1[d] = mfma(xa[1], f, z);
    }
    const int n = lane & 15, g = lane >> 4, kk = n >> 2, c2 = n & 3, h1 = g >> 1;
    const bool hi = (kk >> 1) != 0;
    LAS float* dst = (LAS float*)(L + LDS_P1) + ((w * 2 + h1) * 2 + (g & 1)) * 128 + c2 * 32;
#pragma unroll
    for (int d = 0; d < 8; ++d) {
      f32x4 v;
#pragma unroll
      for (int r = 0; r < 4; ++r) { const float t = hi ? a1[d][r] : a0[d][r]; v[r] = t + dpp<DPP_ROR8>(t); }
      if (kk == h1) *(LAS f32x4*)(dst + d * 4) = v;
    }
  }

  RAW_BARRIER();
  if (tid < 128) {
    const int b = tid >> 4, pr = tid & 15;
    float q0 = 0.f, q1 = 0.f;
    const int c0 = 2 * pr;
    const LAS float* src = (const LAS float*)(L + LDS_P1) + (b >> 2) * 128 + (c0 >> 3) * 32 + (c0 & 7) * 4 + (b & 3);
#pragma unroll
    for (int u = 0; u < 8; ++u) { q0 += src[u * 256]; q1 += src[u * 256 + 4]; }
    st_granule(p.ws, Q_OFF + ((br1 * 8 + b) * 2048 + kvh * 256 + cg * 16 + pr) * 8, pk(q0, q1), tag);
  }

  SB();

  const int hh = jj >> 3, br3 = (jj >> 2) & 1, qt = jj & 3;
#pragma unroll
  for (int j = 0; j < 8; ++j) { WAITVM(PACE); V0[j] = ldV(vA, lo, j); SB(); }
#pragma unroll
  for (int j = 0; j < 8; ++j) { WAITVM(PACE); K1[j] = ldK(kA + 16 * TB, lo, j); SB(); }
#pragma unroll
  for (int j = 0; j < 8; ++j) { WAITVM(PACE); V1[j] = ldV(vA + 16 * TB, lo, j); SB(); }
  const int qoff = Q_OFF + (((w >> 1) * 8 + b2) * 2048 + kv2 * 256 + (w & 1) * 128 + 2 * lane) * 8;
  v4u qg;
  {
    WAITVM(PACE);
    asm volatile("buffer_load_dwordx4 %0, %1, %2, 0 offen sc1" : "=v"(qg) : "v"(qoff), "s"(rs) : "memory");
    const GAS char* wb = (const GAS char*)(br3 ? p.woB : p.woA) + (size_t)((kvh * 4 + hh) * 128 + 32 * w + (lane >> 4)) * 1024 + qt * 256;
    const unsigned c0 = ((((lane & 15) >> 1) ^ (lane >> 4)) << 5) | ((lane & 1) << 4);
    const unsigned ldw = (unsigned)(uintptr_t)(L + LDS_WO) + 32 * w * 256;
#pragma unroll
    for (int i = 0; i < 8; ++i) { WAITVM(PACE); dma16(wb + i * 4096 + (c0 ^ (((i >> 1) & 1) << 7)), ldw + i * 1024); }
    asm volatile("s_waitcnt vmcnt(8)" : "+v"(qg) :: "memory");
  }

  {
    if (!__all(qg.y == tag && qg.w == tag)) {
      unsigned spins = 0;
      do {
        __builtin_amdgcn_s_sleep(2);
        asm volatile("" ::: "memory");
        qg = ldsc(rs, qoff);
      } while (!__all(qg.y == tag && qg.w == tag) && ++spins < kSpinCap);
    }
    const int col = (w & 1) * 256 + 4 * lane;
    *(LAS v2u*)(L + LDS_Q + (w >> 1) * QBR + (col >> 7) * QROW + (col & 127) * 2) = v2u{qg.x, qg.z};
  }

  RAW_BARRIER();

  v4u qf[4];
  f32x4 acc[8];
  float m, l;
  const int qlane = (lane & 3) * QROW + (lane >> 4) * 16;
#pragma unroll
  for (int st = 0; st < 4; ++st) qf[st] = *(const LAS v4u*)(L + LDS_Q + qlane + st * 64);
#pragma unroll
  for (int d = 0; d < 8; ++d) acc[d] = f32x4{0.f, 0.f, 0.f, 0.f};
  m = -INFINITY; l = 0.f;
  f32x4 s0, s1;
#define STEP(K, V, nk, nv) do { qk_part(K, qf, s0, s1); fillK(K, nk, lo); sv_part(V, s0, s1, acc, m, l); fillV(V, nv, lo); } while (0)
  STEP(K0, V0, kB, vB);
  STEP(K1, V1, kB + 16 * TB, vB + 16 * TB);
  park_partial(L, 0, w, lane, acc, m, l);

#pragma unroll
  for (int st = 0; st < 4; ++st) qf[st] = *(const LAS v4u*)(L + LDS_Q + QBR + qlane + st * 64);
#pragma unroll
  for (int d = 0; d < 8; ++d) acc[d] = f32x4{0.f, 0.f, 0.f, 0.f};
  m = -INFINITY; l = 0.f;
  STEP(K0, V0, kB + 32 * TB, vB + 32 * TB);
  STEP(K1, V1, kB + 48 * TB, vB + 48 * TB);
  STEP(K0, V0, kB + 64 * TB, vB + 64 * TB);
  STEP(K1, V1, kB + 80 * TB, vB + 80 * TB);
  STEP(K0, V0, kB + 96 * TB, vB + 96 * TB);
  STEP(K1, V1, kB + 112 * TB, vB + 112 * TB);
  qk_part(K0, qf, s0, s1); sv_part(V0, s0, s1, acc, m, l);
  qk_part(K1, qf, s0, s1); sv_part(V1, s0, s1, acc, m, l);

  park_partial(L, 1, w, lane, acc, m, l);

  WAITVM(0);
  RAW_BARRIER();

  merge_publish(L, p.ws, 1, w, lane, ((8 + kv2) * 4 + w) * 32 + b2 * 4 + s2, tag);
  merge_publish(L, p.ws, 0, w, lane, (kv2 * 4 + w) * 32 + b2 * 4 + s2, tag);

  {
    const int b = tid >> 5, dq = tid & 31;
    const int slot = ((br3 * 8 + kvh) * 4 + hh) * 32 + b * 4;
    v4u pa[4], la0, la1;
    unsigned spins = 0;
    for (;;) {
      asm volatile("" ::: "memory");
      la0 = ldsc(rs, L_OFF + slot * 8);
      la1 = ldsc(rs, L_OFF + slot * 8 + 16);
      const int nv = (la0.y == tag) + (la0.w == tag) + (la1.y == tag) + (la1.w == tag);
      if (__all(nv >= 3) || ++spins > kSpinCap) break;
      __builtin_amdgcn_s_sleep(6);
    }

    bool ok[4] = {false, false, false, false};
    bool lok = la0.y == tag && la0.w == tag && la1.y == tag && la1.w == tag;
    for (;;) {
      asm volatile("" ::: "memory");
#pragma unroll
      for (int s = 0; s < 4; ++s) if (!ok[s]) pa[s] = ldsc(rs, P_OFF + ((slot + s) * 64 + 2 * dq) * 8);
      if (!lok) { la0 = ldsc(rs, L_OFF + slot * 8); la1 = ldsc(rs, L_OFF + slot * 8 + 16); }
#pragma unroll
      for (int s = 0; s < 4; ++s) ok[s] = pa[s].y == tag && pa[s].w == tag;
      lok = la0.y == tag && la0.w == tag && la1.y == tag && la1.w == tag;
      if (__all(ok[0] && ok[1] && ok[2] && ok[3] && lok) || ++spins > kSpinCap) break;
      __builtin_amdgcn_s_sleep(2);
    }

    const float l0 = bc<float>(la0.x), l1 = bc<float>(la0.z), l2 = bc<float>(la1.x), l3 = bc<float>(la1.z);
    const float M = fmaxf(fmaxf(l0, l1), fmaxf(l2, l3));
    const float ws_[4] = {__builtin_amdgcn_exp2f(l0 - M), __builtin_amdgcn_exp2f(l1 - M), __builtin_amdgcn_exp2f(l2 - M), __builtin_amdgcn_exp2f(l3 - M)};
    const float inv = 1.0f / ((ws_[0] + ws_[1]) + (ws_[2] + ws_[3]));
    float o[4] = {0.f, 0.f, 0.f, 0.f};
#pragma unroll
    for (int s = 0; s < 4; ++s) {
      o[0] += ws_[s] * blo(pa[s].x); o[1] += ws_[s] * bhi(pa[s].x);
      o[2] += ws_[s] * blo(pa[s].z); o[3] += ws_[s] * bhi(pa[s].z);
    }
    *(LAS v2u*)(L + LDS_OB + b * 272 + dq * 8) = v2u{pk(o[0] * inv, o[1] * inv), pk(o[2] * inv, o[3] * inv)};
  }
  RAW_BARRIER();
  {
    const int n = lane & 15, g = lane >> 4;
    f32x4 y[2] = {f32x4{0.f, 0.f, 0.f, 0.f}, f32x4{0.f, 0.f, 0.f, 0.f}};
#pragma unroll
    for (int st = 0; st < 4; ++st) {
      const v4u a = *(const LAS v4u*)(L + LDS_OB + (n & 7) * 272 + st * 64 + g * 16);
      const int row = 32 * st + 8 * g + (n >> 2), swz = (n >> 2) | ((g & 1) << 2);
#pragma unroll
      for (int t = 0; t < 2; ++t) {
        const LAS char* wp = L + LDS_WO + row * 256 + (((2 * w + t) ^ swz) << 5) + (n & 3) * 8;
        const s16x4 b0 = __builtin_amdgcn_ds_read_tr16_b64_v4i16((LAS s16x4*)wp);
        const s16x4 b1 = __builtin_amdgcn_ds_read_tr16_b64_v4i16((LAS s16x4*)(wp + 4 * 256));
        const v2u u0 = bc<v2u>(b0), u1 = bc<v2u>(b1);
        y[t] = mfma(a, v4u{u0.x, u0.y, u1.x, u1.y}, y[t]);
      }
    }
    if (g < 2) {
#pragma unroll
      for (int t = 0; t < 2; ++t)
#pragma unroll
        for (int r = 0; r < 4; ++r)
          st_granule(p.ws, Y_OFF + ((((br3 * 32 + qt * 8 + 2 * w + t) * 8 + 4 * g + r) * 32 + kvh * 4 + hh) * 16 + n) * 8, bc<unsigned>(y[t][r]), tag);
    }
  }

  if (w < 2) {
    const int br = 1 - w, np = lane & 7, pl = lane >> 3;
    const int yoff = Y_OFF + ((((br * 32 + jj) * 8 + kvh) * 32 + pl) * 16 + 2 * np) * 8;
    v4u ya[4];
    bool ok[4] = {false, false, false, false};
    unsigned spins = 0;
    for (;;) {
      asm volatile("" ::: "memory");
#pragma unroll
      for (int i = 0; i < 4; ++i) if (!ok[i]) ya[i] = ldsc(rs, yoff + i * 8 * 128);
#pragma unroll
      for (int i = 0; i < 4; ++i) ok[i] = ya[i].y == tag && ya[i].w == tag;
      if (__all(ok[0] && ok[1] && ok[2] && ok[3]) || ++spins > kSpinCap) break;
      __builtin_amdgcn_s_sleep(4);
    }

    float v0 = (bc<float>(ya[0].x) + bc<float>(ya[1].x)) + (bc<float>(ya[2].x) + bc<float>(ya[3].x));
    float v1 = (bc<float>(ya[0].z) + bc<float>(ya[1].z)) + (bc<float>(ya[2].z) + bc<float>(ya[3].z));
    v0 += dpp<DPP_ROR8>(v0); v1 += dpp<DPP_ROR8>(v1);
    v0 = xrow_sum(v0); v1 = xrow_sum(v1);
    if (lane < 8) *(GAS unsigned*)((GAS char*)p.out + (kvh * 1024 + br * 512 + jj * 16 + 2 * np) * 2) = pk(v0, v1);
  }

  if (wg == 0 && tid == 0) __hip_atomic_store((gu32*)(p.ws + EPOCH_OFF), tag, __ATOMIC_RELAXED, __HIP_MEMORY_SCOPE_AGENT);
}

}

extern "C" void kernel_launch(void* const* d_in, const int* in_sizes, int n_in, void* d_out, int out_size, void* d_ws,
                              size_t ws_size, hipStream_t stream) {
  (void)in_sizes; (void)n_in; (void)out_size; (void)ws_size;
  Params p;
  p.xA = (const char*)d_in[0]; p.kA = (const char*)d_in[1]; p.vA = (const char*)d_in[2]; p.wqA = (const char*)d_in[3]; p.woA = (const char*)d_in[4];
  p.xB = (const char*)d_in[5]; p.kB = (const char*)d_in[6]; p.vB = (const char*)d_in[7]; p.wqB = (const char*)d_in[8]; p.woB = (const char*)d_in[9];
  p.out = (char*)d_out; p.ws = (char*)d_ws;
  (void)hipFuncSetAttribute((const void*)fused_decode, hipFuncAttributeMaxDynamicSharedMemorySize, LDS_BYTES);
  hipLaunchKernelGGL(fused_decode, dim3(256), dim3(256), LDS_BYTES, stream, p);
}
```

```cpp
#include <hip/hip_runtime.h>
#include <stdint.h>
#define PACE 7
#define WE 6
#define WO 6
#define SA 740
#define TAU 228

#define SL1 6
#define SL2 2
#define SL3 4
#define GAS __attribute__((address_space(1)))
#define LAS __attribute__((address_space(3)))
typedef unsigned v4u __attribute__((ext_vector_type(4)));
typedef unsigned v2u __attribute__((ext_vector_type(2)));
typedef float f32x4 __attribute__((ext_vector_type(4)));
typedef __bf16 bf16x8 __attribute__((ext_vector_type(8)));
typedef __bf16 bf16x2 __attribute__((ext_vector_type(2)));
typedef short s16x4 __attribute__((ext_vector_type(4)));
typedef GAS unsigned long long gu64;
typedef GAS unsigned gu32;

namespace {

constexpr int SK = 4096, DH = 128;
constexpr float SCL = 0.08838834764831845f * 1.4426950408889634f;
constexpr unsigned kSpinCap = 1u << 14;

constexpr int EPOCH_OFF = 0;
constexpr int T_OFF = 1024;
constexpr int Q_OFF = 4096, Q_BYTES = 2 * 8 * 2048 * 8;
constexpr int P_OFF = Q_OFF + Q_BYTES, P_BYTES = 2 * 8 * 4 * 8 * 4 * 64 * 8;
constexpr int L_OFF = P_OFF + P_BYTES, L_BYTES = 2 * 8 * 4 * 8 * 4 * 8;
constexpr int Y_OFF = L_OFF + L_BYTES, Y_BYTES = 2 * 32 * 8 * 32 * 16 * 8;
constexpr int WS_BYTES = Y_OFF + Y_BYTES;

constexpr int LDS_P1 = 0;
constexpr int LDS_Q = 8192;
constexpr int QROW = 272, QBR = 4 * QROW;
constexpr int LDS_ML = LDS_Q + 2 * QBR + 128;
constexpr int LDS_MRG = LDS_ML + 256;
constexpr int MROW = 132;
constexpr int LDS_WO = LDS_MRG + 2 * 4 * 4 * MROW * 4;
constexpr int LDS_KT = LDS_WO;
constexpr int LDS_OB = LDS_WO + 32768;
constexpr int LDS_RED = LDS_OB + 8 * 272;
constexpr int LDS_CNT = LDS_RED;
constexpr int LDS_END = LDS_CNT + 16;
constexpr int LDS_BYTES = 84 * 1024;
static_assert(LDS_END <= LDS_BYTES && LDS_KT % 256 == 0, "lds");
static_assert(LDS_ML % 16 == 0 && LDS_MRG % 16 == 0 && LDS_WO % 16 == 0 && LDS_OB % 16 == 0 && LDS_RED % 16 == 0, "align");

struct Params {
  const char* xA; const char* kA; const char* vA; const char* wqA; const char* woA;
  const char* xB; const char* kB; const char* vB; const char* wqB; const char* woB;
  char* out; char* ws;
};

template <typename T, typename F> __device__ __forceinline__ T bc(F x) { return __builtin_bit_cast(T, x); }
__device__ __forceinline__ f32x4 mfma(v4u a, v4u b, f32x4 c) {
  return __builtin_amdgcn_mfma_f32_16x16x32_bf16(bc<bf16x8>(a), bc<bf16x8>(b), c, 0, 0, 0);
}
__device__ __forceinline__ unsigned pk(float lo, float hi) { bf16x2 r = {(__bf16)lo, (__bf16)hi}; return bc<unsigned>(r); }
__device__ __forceinline__ float blo(unsigned u) { return bc<float>(u << 16); }
__device__ __forceinline__ float bhi(unsigned u) { return bc<float>(u & 0xffff0000u); }
__device__ __forceinline__ v4u ldnt(const GAS char* p) { return __builtin_nontemporal_load((const GAS v4u*)p); }
__device__ __forceinline__ v4u ldsc(__amdgpu_buffer_rsrc_t rs, int off) { return __builtin_amdgcn_raw_buffer_load_b128(rs, off, 0, 16); }
__device__ __forceinline__ void st_granule(char* ws, int off, unsigned val, unsigned tag) {
  __hip_atomic_store((gu64*)(ws + off), ((unsigned long long)tag << 32) | val, __ATOMIC_RELAXED, __HIP_MEMORY_SCOPE_AGENT);
}
__device__ __forceinline__ void st_buf8(__amdgpu_buffer_rsrc_t rs, int off, unsigned val, unsigned tag) {
  __builtin_amdgcn_raw_buffer_store_b64(v2u{val, tag}, rs, off, 0, 16);
}
__device__ __forceinline__ void st_buf16(__amdgpu_buffer_rsrc_t rs, int off, unsigned v0, unsigned v1, unsigned tag) {
  __builtin_amdgcn_raw_buffer_store_b128(v4u{v0, tag, v1, tag}, rs, off, 0, 16);
}
constexpr int OOB = 0x7ffffff0;
__device__ __forceinline__ unsigned now() { return (unsigned)__builtin_amdgcn_s_memrealtime(); }
__device__ __forceinline__ void lds_wait_count(unsigned addr, unsigned n) {
  unsigned t;
  asm volatile("1:\n\tds_read_b32 %0, %1\n\ts_waitcnt lgkmcnt(0)\n\tv_readfirstlane_b32 s90, %0\n\ts_cmp_lt_u32 s90, %2\n\t"
               "s_cbranch_scc0 2f\n\ts_sleep 1\n\ts_branch 1b\n2:" : "=&v"(t) : "v"(addr), "s"(n) : "s90", "scc", "memory");
}
__device__ __forceinline__ void win_wait(unsigned win) {
  asm volatile("s_cmp_gt_u32 %0, 8\n\ts_cbranch_scc1 1f\n\ts_waitcnt vmcnt(7)\n\t"
               "s_cmp_gt_u32 %0, 7\n\ts_cbranch_scc1 1f\n\ts_waitcnt vmcnt(6)\n\t"
               "s_cmp_gt_u32 %0, 6\n\ts_cbranch_scc1 1f\n\ts_waitcnt vmcnt(5)\n\t"
               "s_cmp_gt_u32 %0, 5\n\ts_cbranch_scc1 1f\n\ts_waitcnt vmcnt(4)\n\t"
               "s_cmp_gt_u32 %0, 4\n\ts_cbranch_scc1 1f\n\ts_waitcnt vmcnt(3)\n1:" :: "s"(win) : "scc", "memory");
}
#define L1 40
#define L2 100
#define L3 160
#define LM1 40
#define LM2 100000
__device__ __forceinline__ unsigned pick_win(unsigned sched, unsigned lo_) {
  unsigned wv;
  asm volatile("s_memrealtime s[90:91]\n\ts_waitcnt lgkmcnt(0)\n\ts_sub_u32 s90, s90, %1\n\ts_mov_b32 %0, %2\n\t"
               "s_cmp_gt_i32 s90, %7\n\ts_addc_u32 %0, %0, 0\n\t"
               "s_cmp_gt_i32 s90, %3\n\ts_addc_u32 %0, %0, 0\n\ts_cmp_gt_i32 s90, %4\n\ts_addc_u32 %0, %0, 0\n\t"
               "s_cmp_gt_i32 s90, %5\n\ts_addc_u32 %0, %0, 0\n\ts_cmp_gt_i32 s90, %6\n\ts_cselect_b32 %0, 16, %0"
               : "=&s"(wv) : "s"(sched), "s"(lo_), "i"(-LM1), "i"(L1), "i"(L2), "i"(L3), "i"(-LM2) : "s90", "s91", "scc");
  return wv;
}
#define SB() __builtin_amdgcn_sched_barrier(0)
#define WAITVM_(n) asm volatile("s_waitcnt vmcnt(" #n ")" ::: "memory")
#define WAITVM(n) WAITVM_(n)
#define RAW_BARRIER() asm volatile("s_waitcnt lgkmcnt(0)\n\ts_barrier" ::: "memory")

__device__ __forceinline__ v4u tcol(const v4u (&r)[8], int d) {
  const unsigned sel = (d & 1) ? 0x07060302u : 0x05040100u;
  const int c = d >> 1;
  v4u f;
  f.x = __builtin_amdgcn_perm(r[1][c], r[0][c], sel);
  f.y = __builtin_amdgcn_perm(r[3][c], r[2][c], sel);
  f.z = __builtin_amdgcn_perm(r[5][c], r[4][c], sel);
  f.w = __builtin_amdgcn_perm(r[7][c], r[6][c], sel);
  return f;
}
__device__ __forceinline__ float xrow_max(float x) {
  auto s = __builtin_amdgcn_permlane16_swap(bc<unsigned>(x), bc<unsigned>(x), false, false);
  x = fmaxf(bc<float>(s[0]), bc<float>(s[1]));
  auto t = __builtin_amdgcn_permlane32_swap(bc<unsigned>(x), bc<unsigned>(x), false, false);
  return fmaxf(bc<float>(t[0]), bc<float>(t[1]));
}
__device__ __forceinline__ float xrow_sum(float x) {
  auto s = __builtin_amdgcn_permlane16_swap(bc<unsigned>(x), bc<unsigned>(x), false, false);
  x = bc<float>(s[0]) + bc<float>(s[1]);
  auto t = __builtin_amdgcn_permlane32_swap(bc<unsigned>(x), bc<unsigned>(x), false, false);
  return bc<float>(t[0]) + bc<float>(t[1]);
}
template <int CTRL> __device__ __forceinline__ float dpp(float x) {
  return bc<float>(__builtin_amdgcn_mov_dpp(bc<int>(x), CTRL, 0xf, 0xf, true));
}
constexpr int DPP_ROR8 = 0x128;

struct LaneOff { unsigned v0, v1, kw, kr; };
__device__ __forceinline__ v4u ldV(const GAS char* t, const LaneOff& o, int j) { return ldnt(t + ((j < 4) ? o.v0 : o.v1) + (j & 3) * 1024); }

__device__ __forceinline__ void k_park(const v4u (&K)[8], LAS char* L, const LaneOff& o) {
#pragma unroll
  for (int j = 0; j < 8; ++j) *(LAS v4u*)(L + (o.kw ^ ((j & 3) << 4)) + j * 1024) = K[j];
  SB();
}
__device__ __forceinline__ void qk_part(LAS char* L, const LaneOff& o, const v4u (&qf)[4], f32x4& s0, f32x4& s1) {
  s0 = f32x4{0.f, 0.f, 0.f, 0.f}; s1 = s0;
#pragma unroll
  for (int st = 0; st < 4; ++st) {
    const LAS char* a = L + (o.kr ^ ((st & 1) << 6)) + (st >> 1) * 128;
    s0 = mfma(*(const LAS v4u*)a, qf[st], s0);
    s1 = mfma(*(const LAS v4u*)(a + 4096), qf[st], s1);
  }
  SB();
}
__device__ __forceinline__ void sv_part(const v4u (&V)[8], const f32x4& s0, const f32x4& s1, f32x4 (&acc)[8], float& m, float& l) {
  float t = fmaxf(fmaxf(fmaxf(s0[0], s0[1]), fmaxf(s0[2], s0[3])), fmaxf(fmaxf(s1[0], s1[1]), fmaxf(s1[2], s1[3])));
  t = xrow_max(t);
  const float mn = fmaxf(m, t);
  const float alpha = __builtin_amdgcn_exp2f((m - mn) * SCL);
  const float mc = mn * SCL;
  float p[8];
#pragma unroll
  for (int r = 0; r < 4; ++r) {
    p[r] = __builtin_amdgcn_exp2f(s0[r] * SCL - mc);
    p[4 + r] = __builtin_amdgcn_exp2f(s1[r] * SCL - mc);
  }
  l = l * alpha + (((p[0] + p[1]) + (p[2] + p[3])) + ((p[4] + p[5]) + (p[6] + p[7])));
  m = mn;
  const v4u P = {pk(p[0], p[1]), pk(p[2], p[3]), pk(p[4], p[5]), pk(p[6], p[7])};
#pragma unroll
  for (int d = 0; d < 8; ++d) {
    acc[d] *= alpha;
    acc[d] = mfma(tcol(V, d), P, acc[d]);
  }
  SB();
}

template <bool LD>
__device__ __forceinline__ void qk_park_ld(LAS char* L, const LaneOff& o, const v4u (&qf)[4], f32x4& s0, f32x4& s1, const v4u (&K)[8],
                                           v4u (&D)[8], const GAS char* t, unsigned win) {
  s0 = f32x4{0.f, 0.f, 0.f, 0.f}; s1 = s0;
#pragma unroll
  for (int st = 0; st < 4; ++st) {
    if (LD) { win_wait(win); D[st] = ldV(t, o, st); SB(); }
    const LAS char* a = L + (o.kr ^ ((st & 1) << 6)) + (st >> 1) * 128;
    s0 = mfma(*(const LAS v4u*)a, qf[st], s0);
    s1 = mfma(*(const LAS v4u*)(a + 4096), qf[st], s1);
    SB();
  }
#pragma unroll
  for (int j = 0; j < 8; ++j) {
    if (LD && (j & 1) == 0) { win_wait(win); D[4 + j / 2] = ldV(t, o, 4 + j / 2); SB(); }
    *(LAS v4u*)(L + (o.kw ^ ((j & 3) << 4)) + j * 1024) = K[j];
    if (LD && (j & 1)) SB();
  }
  SB();
}
template <bool LD>
__device__ __forceinline__ void sv_ld(const v4u (&V)[8], const f32x4& s0, const f32x4& s1, f32x4 (&acc)[8], float& m, float& l,
                                      v4u (&D)[8], const GAS char* t, const LaneOff& o, unsigned win) {
  if (LD) { win_wait(win); D[0] = ldV(t, o, 0); SB(); }
  float tm = fmaxf(fmaxf(fmaxf(s0[0], s0[1]), fmaxf(s0[2], s0[3])), fmaxf(fmaxf(s1[0], s1[1]), fmaxf(s1[2], s1[3])));
  tm = xrow_max(tm);
  const float mn = fmaxf(m, tm);
  const float alpha = __builtin_amdgcn_exp2f((m - mn) * SCL);
  const float mc = mn * SCL;
  if (LD) { SB(); win_wait(win); D[1] = ldV(t, o, 1); SB(); }
  float p[8];
#pragma unroll
  for (int r = 0; r < 4; ++r) {
    p[r] = __builtin_amdgcn_exp2f(s0[r] * SCL - mc);
    p[4 + r] = __builtin_amdgcn_exp2f(s1[r] * SCL - mc);
  }
  l = l * alpha + (((p[0] + p[1]) + (p[2] + p[3])) + ((p[4] + p[5]) + (p[6] + p[7])));
  m = mn;
  const v4u P = {pk(p[0], p[1]), pk(p[2], p[3]), pk(p[4], p[5]), pk(p[6], p[7])};
#pragma unroll
  for (int d = 0; d < 8; ++d) {
    if (LD && d < 6) { SB(); win_wait(win); D[2 + d] = ldV(t, o, 2 + d); SB(); }
    acc[d] *= alpha;
    acc[d] = mfma(tcol(V, d), P, acc[d]);
  }
  SB();
}

__device__ __forceinline__ void park_partial(LAS char* L, int br, int w, int lane, const f32x4 (&acc)[8], float m, float l) {
  const float ls = xrow_sum(l);
  const int n = lane & 15, g = lane >> 4;
  if (n < 4) {
    LAS float* o = (LAS float*)(L + LDS_MRG) + ((br * 4 + w) * 4 + n) * MROW + g * 32;
#pragma unroll
    for (int d = 0; d < 8; ++d) *(LAS f32x4*)(o + d * 4) = acc[d];
    if (g == 0) { LAS float* ml = (LAS float*)(L + LDS_ML) + ((br * 4 + w) * 4 + n) * 2; ml[0] = m; ml[1] = ls; }
  }
}

__device__ __forceinline__ void merge(LAS char* L, int br, int h, int dp, unsigned& val, unsigned& lse) {
  const int D = 2 * dp, idx = (D >> 5) * 32 + (D & 7) * 4 + ((D >> 3) & 3);
  const LAS float* ml = (const LAS float*)(L + LDS_ML) + (br * 16 + h) * 2;
  float mw[4], lw[4], a0[4], a1[4];
#pragma unroll
  for (int u = 0; u < 4; ++u) {
    const LAS float* o = (const LAS float*)(L + LDS_MRG) + ((br * 4 + u) * 4 + h) * MROW + idx;
    mw[u] = ml[u * 8]; lw[u] = ml[u * 8 + 1]; a0[u] = o[0]; a1[u] = o[4];
  }
  const float M = fmaxf(fmaxf(mw[0], mw[1]), fmaxf(mw[2], mw[3]));
  float Ls = 0.f, o0 = 0.f, o1 = 0.f;
#pragma unroll
  for (int u = 0; u < 4; ++u) {
    const float f = __builtin_amdgcn_exp2f((mw[u] - M) * SCL);
    Ls += f * lw[u]; o0 += f * a0[u]; o1 += f * a1[u];
  }
  const float inv = __builtin_amdgcn_rcpf(Ls);
  val = pk(o0 * inv, o1 * inv);
  lse = bc<unsigned>(M * SCL + __builtin_amdgcn_logf(Ls));
}
__device__ __forceinline__ void publish(__amdgpu_buffer_rsrc_t rs, int dp, int slot, unsigned val, unsigned lse, unsigned tag) {
  st_buf8(rs, P_OFF + (slot * 64 + dp) * 8, val, tag);
  st_buf8(rs, dp == 0 ? L_OFF + slot * 8 : OOB, lse, tag);
}

__global__ void __launch_bounds__(256, 2) fused_decode(Params p) {
  extern __shared__ __attribute__((aligned(16))) char smem[];
  LAS char* L = (LAS char*)smem;
  const int tid = threadIdx.x, lane = tid & 63, w = __builtin_amdgcn_readfirstlane(tid >> 6);
  const int wg = blockIdx.x, kvh = wg & 7, jj = wg >> 3;
  const __amdgpu_buffer_rsrc_t rs = __builtin_amdgcn_make_buffer_rsrc(p.ws, 0, WS_BYTES, 0x00020000);

  const unsigned t_entry = now();
  const unsigned tag = max(__hip_atomic_load((gu32*)(p.ws + EPOCH_OFF), __ATOMIC_RELAXED, __HIP_MEMORY_SCOPE_AGENT) + 1u, 1u);
  const int br1 = jj >> 4;
  const int kv1 = kvh, cg = jj & 15;
  const int s2 = kvh >> 1, pr2 = (((jj - 3 * s2) & 31) << 1) | (kvh & 1), b2 = pr2 >> 3, kv2 = pr2 & 7;
  const size_t pair = (size_t)(b2 * 8 + kv2) * SK * 256;
  LaneOff lo;
  {
    const int i = lane & 15, g = lane >> 4;
    lo.v0 = (unsigned)lane * 16u;
    lo.v1 = lo.v0 + 4096u;
    lo.kw = (unsigned)(LDS_KT + w * 8192 + g * 256 + ((i ^ ((g & 1) << 2)) << 4));
    lo.kr = (unsigned)(LDS_KT + w * 8192 + (4 * (i & 3) + (i >> 2)) * 256 + ((g ^ (i & 7)) << 4));
  }
  const int g16 = s2 * 4 + w;
  constexpr int TB = 32 * 256;
  const GAS char* kA = (const GAS char*)p.kA + pair + (size_t)(3072 * 256 + g16 * TB);
  const GAS char* vA = (const GAS char*)p.vA + pair + (size_t)(3072 * 256 + g16 * TB);
  const GAS char* kB = (const GAS char*)p.kB + pair + (size_t)(g16 * TB);
  const GAS char* vB = (const GAS char*)p.vB + pair + (size_t)(g16 * TB);

  v4u xa[2], wv[8];
  v4u K0[8], V0[8], K1[8], V1[8];
  {
    const GAS char* x = (const GAS char*)(br1 ? p.xB : p.xA);
    const int m = lane & 15, g = lane >> 4, h1 = m >> 3, b = m & 7;
#pragma unroll
    for (int ps = 0; ps < 2; ++ps) xa[ps] = *(const GAS v4u*)(x + (b * 512 + 128 * w + 8 * (4 * g + 2 * ps + h1)) * 2);
    const GAS char* wq = (const GAS char*)(br1 ? p.wqB : p.wqA) + (size_t)(128 * w) * 8192 + (kv1 * 512 + 32 * cg) * 2;
    const unsigned wo_ = (unsigned)(8 * (lane >> 2)) * 8192u + (lane & 3) * 16u;
#pragma unroll
    for (int j = 0; j < 8; ++j) wv[j] = ldnt(wq + wo_ + j * 8192u);
    SB();
    WAITVM(7);
#pragma unroll
    for (int j = 0; j < 8; ++j) K0[j] = ldV(kA, lo, j);
  }
  SB();

  {
    f32x4 a0[8], a1[8];
#pragma unroll
    for (int d = 0; d < 8; ++d) {
      const v4u f = tcol(wv, d);
      const f32x4 z = {0.f, 0.f, 0.f, 0.f};
      a0[d] = mfma(xa[0], f, z);
      a1[d] = mfma(xa[1], f, z);
    }
    const int n = lane & 15, g = lane >> 4, kk = n >> 2, c2 = n & 3, h1 = g >> 1;
    const bool hi = (kk >> 1) != 0;
    LAS float* dst = (LAS float*)(L + LDS_P1) + ((w * 2 + h1) * 2 + (g & 1)) * 128 + c2 * 32;
#pragma unroll
    for (int d = 0; d < 8; ++d) {
      f32x4 v;
#pragma unroll
      for (int r = 0; r < 4; ++r) { const float t = hi ? a1[d][r] : a0[d][r]; v[r] = t + dpp<DPP_ROR8>(t); }
      if (kk == h1) *(LAS f32x4*)(dst + d * 4) = v;
    }
  }

  if (tid == 0) *(LAS unsigned*)(L + LDS_CNT) = 0u;
  RAW_BARRIER();
  if (wg < 8 && tid == 128) st_granule(p.ws, T_OFF + wg * 8, t_entry, tag);
  if (tid < 128) {
    const int b = tid >> 4, pr = tid & 15;
    float q0 = 0.f, q1 = 0.f;
    const int c0 = 2 * pr;
    const LAS float* src = (const LAS float*)(L + LDS_P1) + (b >> 2) * 128 + (c0 >> 3) * 32 + (c0 & 7) * 4 + (b & 3);
#pragma unroll
    for (int u = 0; u < 8; ++u) { q0 += src[u * 256]; q1 += src[u * 256 + 4]; }
    st_granule(p.ws, Q_OFF + ((br1 * 8 + b) * 2048 + kv1 * 256 + cg * 16 + pr) * 8, pk(q0, q1), tag);
  }

  SB();

#pragma unroll
  for (int j = 0; j < 8; ++j) { WAITVM(PACE); V0[j] = ldV(vA, lo, j); SB(); }
#pragma unroll
  for (int j = 0; j < 8; ++j) { WAITVM(PACE); K1[j] = ldV(kA + 16 * TB, lo, j); SB(); }
#pragma unroll
  for (int j = 0; j < 8; ++j) { WAITVM(PACE); V1[j] = ldV(vA + 16 * TB, lo, j); SB(); }
  k_park(K0, L, lo);
  const int qoff = Q_OFF + (((w >> 1) * 8 + b2) * 2048 + kv2 * 256 + (w & 1) * 128 + 2 * lane) * 8;
  const int toff = T_OFF + (lane & 3) * 16;
  v4u qg, tg;
  WAITVM(PACE);
  qg = ldsc(rs, qoff);
  tg = ldsc(rs, toff);
  SB();
#pragma unroll
  for (int j = 0; j < 8; ++j) { WAITVM(PACE); K0[j] = ldV(kB, lo, j); SB(); }

  unsigned S0;
  {
    if (!__all(qg.y == tag && qg.w == tag && tg.y == tag && tg.w == tag)) {
      unsigned spins = 0;
      do {
        __builtin_amdgcn_s_sleep(2);
        asm volatile("" ::: "memory");
        qg = ldsc(rs, qoff);
        tg = ldsc(rs, toff);
      } while (!__all(qg.y == tag && qg.w == tag && tg.y == tag && tg.w == tag) && ++spins < kSpinCap);
    }
    const int col = (w & 1) * 256 + 4 * lane;
    *(LAS v2u*)(L + LDS_Q + (w >> 1) * QBR + (col >> 7) * QROW + (col & 127) * 2) = v2u{qg.x, qg.z};
    int d = min((int)(tg.x - t_entry), (int)(tg.z - t_entry));
    d = min(d, __builtin_amdgcn_mov_dpp(d, 0xb1, 0xf, 0xf, true));
    d = min(d, __builtin_amdgcn_mov_dpp(d, 0x4e, 0xf, 0xf, true));
    S0 = t_entry + (unsigned)__builtin_amdgcn_readfirstlane(d) + SA;
  }

  RAW_BARRIER();

  v4u qf[4];
  f32x4 acc[8];
  float m, l;
  const int qlane = (lane & 3) * QROW + (lane >> 4) * 16;
#pragma unroll
  for (int st = 0; st < 4; ++st) qf[st] = *(const LAS v4u*)(L + LDS_Q + qlane + st * 64);
#pragma unroll
  for (int d = 0; d < 8; ++d) acc[d] = f32x4{0.f, 0.f, 0.f, 0.f};
  m = -INFINITY; l = 0.f;
  f32x4 s0, s1;
  const unsigned wbase = (kvh & 1) ? WO : WE;
  unsigned win = wbase;
#define PICK(t) win = pick_win(S0 + (t) * TAU - ((t) ? TAU / 2 : 0), wbase - 2u)
#define STEPI(t, R, nk, V, Vp, nv) do { PICK(t); qk_park_ld<true>(L, lo, qf, s0, s1, R, Vp, nv, win); sv_ld<true>(V, s0, s1, acc, m, l, R, nk, lo, win); } while (0)
  PICK(0); qk_park_ld<false>(L, lo, qf, s0, s1, K1, V1, vB, win); sv_ld<true>(V0, s0, s1, acc, m, l, K1, kB + 16 * TB, lo, win);
  STEPI(1, K0, kB + 32 * TB, V1, V0, vB);
  park_partial(L, 0, w, lane, acc, m, l);
  if (lane == 0) __hip_atomic_fetch_add((LAS unsigned*)(L + LDS_CNT), 1u, __ATOMIC_RELAXED, __HIP_MEMORY_SCOPE_WORKGROUP);

#pragma unroll
  for (int st = 0; st < 4; ++st) qf[st] = *(const LAS v4u*)(L + LDS_Q + QBR + qlane + st * 64);
#pragma unroll
  for (int d = 0; d < 8; ++d) acc[d] = f32x4{0.f, 0.f, 0.f, 0.f};
  m = -INFINITY; l = 0.f;
  unsigned aval = 0, alse = 0;
  STEPI(2, K1, kB + 48 * TB, V0, V1, vB + 16 * TB);
  STEPI(3, K0, kB + 64 * TB, V1, V0, vB + 32 * TB);
  lds_wait_count((unsigned)(uintptr_t)(L + LDS_CNT), 4u);
  merge(L, 0, w, lane, aval, alse);
  SB();
  STEPI(4, K1, kB + 80 * TB, V0, V1, vB + 48 * TB);
  STEPI(5, K0, kB + 96 * TB, V1, V0, vB + 64 * TB);
  STEPI(6, K1, kB + 112 * TB, V0, V1, vB + 80 * TB);
  PICK(7);
  qk_park_ld<true>(L, lo, qf, s0, s1, K0, V0, vB + 96 * TB, win); sv_ld<false>(V1, s0, s1, acc, m, l, K0, kB, lo, win);
  PICK(8);
  qk_park_ld<true>(L, lo, qf, s0, s1, K1, V1, vB + 112 * TB, win);

  publish(rs, lane, (kv2 * 4 + w) * 32 + b2 * 4 + s2, aval, alse, tag);
  SB();
  const int hh = jj >> 3, br3 = (jj >> 2) & 1, qt = jj & 3;
  sv_part(V0, s0, s1, acc, m, l);
  qk_part(L, lo, qf, s0, s1);
  {
    const GAS char* wb = (const GAS char*)(br3 ? p.woB : p.woA) + (size_t)((kvh * 4 + hh) * 128 + 32 * w) * 1024 + qt * 256;
    const unsigned wl = (unsigned)(lane >> 4) * 1024u + (lane & 15) * 16u;
#pragma unroll
    for (int i = 0; i < 8; ++i) K0[i] = ldnt(wb + wl + i * 4096);
    SB();
  }
  sv_part(V1, s0, s1, acc, m, l);

  park_partial(L, 1, w, lane, acc, m, l);

  RAW_BARRIER();

  const int slot3 = ((br3 * 8 + kvh) * 4 + hh) * 32 + (tid >> 5) * 4;
  v4u pa[4], la0, la1;
  {
    unsigned bval, blse;
    merge(L, 1, w, lane, bval, blse);
    publish(rs, lane, ((8 + kv2) * 4 + w) * 32 + b2 * 4 + s2, bval, blse, tag);
  }

  SB();
  {
    LAS char* wd = L + LDS_WO + (32 * w + (lane >> 4)) * 256 + ((lane & 1) << 4);
    const int c = ((lane & 15) >> 1) ^ (lane >> 4);
#pragma unroll
    for (int i = 0; i < 8; ++i) *(LAS v4u*)(wd + i * 1024 + ((c ^ (((i >> 1) & 1) << 2)) << 5)) = K0[i];
  }
  RAW_BARRIER();
  v4u wf[2][4];
  {
    const int n = lane & 15, g = lane >> 4, swz = (n >> 2) | ((g & 1) << 2);
#pragma unroll
    for (int st = 0; st < 4; ++st) {
      const int row = 32 * st + 8 * g + (n >> 2);
#pragma unroll
      for (int t = 0; t < 2; ++t) {
        const LAS char* wp = L + LDS_WO + row * 256 + (((2 * w + t) ^ swz) << 5) + (n & 3) * 8;
        const v2u u0 = bc<v2u>(__builtin_amdgcn_ds_read_tr16_b64_v4i16((LAS s16x4*)wp));
        const v2u u1 = bc<v2u>(__builtin_amdgcn_ds_read_tr16_b64_v4i16((LAS s16x4*)(wp + 4 * 256)));
        wf[t][st] = v4u{u0.x, u0.y, u1.x, u1.y};
      }
    }
  }

  {
    const int b = tid >> 5, dq = tid & 31, slot = slot3;
    unsigned spins = 0;
    bool ok[4] = {false, false, false, false};
    bool lok;
    {
      for (;;) {
        asm volatile("" ::: "memory");
        la0 = ldsc(rs, L_OFF + slot * 8);
        la1 = ldsc(rs, L_OFF + slot * 8 + 16);
        const int nv = (la0.y == tag) + (la0.w == tag) + (la1.y == tag) + (la1.w == tag);
        if (__all(nv >= 3) || ++spins > kSpinCap) break;
        __builtin_amdgcn_s_sleep(SL1);
      }

      lok = la0.y == tag && la0.w == tag && la1.y == tag && la1.w == tag;
      for (;;) {
        asm volatile("" ::: "memory");
#pragma unroll
        for (int s = 0; s < 4; ++s) if (!ok[s]) pa[s] = ldsc(rs, P_OFF + ((slot + s) * 64 + 2 * dq) * 8);
        if (!lok) { la0 = ldsc(rs, L_OFF + slot * 8); la1 = ldsc(rs, L_OFF + slot * 8 + 16); }
#pragma unroll
        for (int s = 0; s < 4; ++s) ok[s] = pa[s].y == tag && pa[s].w == tag;
        lok = la0.y == tag && la0.w == tag && la1.y == tag && la1.w == tag;

        if (__all(ok[0] && ok[1] && ok[2] && ok[3] && lok) || ++spins > kSpinCap) break;
        __builtin_amdgcn_s_sleep(SL2);
      }
    }

    const float l0 = bc<float>(la0.x), l1 = bc<float>(la0.z), l2 = bc<float>(la1.x), l3 = bc<float>(la1.z);
    const float M = fmaxf(fmaxf(l0, l1), fmaxf(l2, l3));
    const float ws_[4] = {__builtin_amdgcn_exp2f(l0 - M), __builtin_amdgcn_exp2f(l1 - M), __builtin_amdgcn_exp2f(l2 - M), __builtin_amdgcn_exp2f(l3 - M)};
    const float inv = __builtin_amdgcn_rcpf((ws_[0] + ws_[1]) + (ws_[2] + ws_[3]));
    float o[4] = {0.f, 0.f, 0.f, 0.f};
#pragma unroll
    for (int s = 0; s < 4; ++s) {
      o[0] += ws_[s] * blo(pa[s].x); o[1] += ws_[s] * bhi(pa[s].x);
      o[2] += ws_[s] * blo(pa[s].z); o[3] += ws_[s] * bhi(pa[s].z);
    }
    *(LAS v2u*)(L + LDS_OB + b * 272 + dq * 8) = v2u{pk(o[0] * inv, o[1] * inv), pk(o[2] * inv, o[3] * inv)};
  }
  RAW_BARRIER();
  {
    const int n = lane & 15, g = lane >> 4;
    f32x4 y[2] = {f32x4{0.f, 0.f, 0.f, 0.f}, f32x4{0.f, 0.f, 0.f, 0.f}};
#pragma unroll
    for (int st = 0; st < 4; ++st) {
      const v4u a = *(const LAS v4u*)(L + LDS_OB + (n & 7) * 272 + st * 64 + g * 16);
      y[0] = mfma(a, wf[0][st], y[0]);
      y[1] = mfma(a, wf[1][st], y[1]);
    }
    const f32x4 yy = (g >> 1) ? y[1] : y[0];
    const bool odd = n & 1;
    const float s0_ = odd ? yy[0] : yy[2], s1_ = odd ? yy[1] : yy[3];
    const float n0 = dpp<0xb1>(s0_), n1 = dpp<0xb1>(s1_);
    const float a0 = odd ? n0 : yy[0], b0 = odd ? yy[2] : n0;
    const float a1 = odd ? n1 : yy[1], b1 = odd ? yy[3] : n1;
    const int r0 = 4 * (g & 1) + (odd ? 2 : 0);
    const int yo = Y_OFF + ((((br3 * 32 + qt * 8 + 2 * w + (g >> 1)) * 8 + r0) * 32 + kvh * 4 + hh) * 16 + (n & ~1)) * 8;
    st_buf16(rs, yo, bc<unsigned>(a0), bc<unsigned>(b0), tag);
    st_buf16(rs, yo + 32 * 16 * 8, bc<unsigned>(a1), bc<unsigned>(b1), tag);
  }

  if (w < 2) {
    const int br = 1 - w, np = lane & 7, pl = lane >> 3;
    const int yoff = Y_OFF + ((((br * 32 + jj) * 8 + kvh) * 32 + pl) * 16 + 2 * np) * 8;
    v4u ya[4];
    bool ok[4] = {false, false, false, false};
    unsigned spins = 0;
    for (;;) {
      asm volatile("" ::: "memory");
#pragma unroll
      for (int i = 0; i < 4; ++i) if (!ok[i]) ya[i] = ldsc(rs, yoff + i * 8 * 128);
#pragma unroll
      for (int i = 0; i < 4; ++i) ok[i] = ya[i].y == tag && ya[i].w == tag;

      if (__all(ok[0] && ok[1] && ok[2] && ok[3]) || ++spins > kSpinCap) break;
      __builtin_amdgcn_s_sleep(SL3);
    }

    float v0 = (bc<float>(ya[0].x) + bc<float>(ya[1].x)) + (bc<float>(ya[2].x) + bc<float>(ya[3].x));
    float v1 = (bc<float>(ya[0].z) + bc<float>(ya[1].z)) + (bc<float>(ya[2].z) + bc<float>(ya[3].z));
    v0 += dpp<DPP_ROR8>(v0); v1 += dpp<DPP_ROR8>(v1);
    v0 = xrow_sum(v0); v1 = xrow_sum(v1);
    if (lane < 8) *(GAS unsigned*)((GAS char*)p.out + (kvh * 1024 + br * 512 + jj * 16 + 2 * np) * 2) = pk(v0, v1);
  }

  if (wg == 0 && tid == 0) __hip_atomic_store((gu32*)(p.ws + EPOCH_OFF), tag, __ATOMIC_RELAXED, __HIP_MEMORY_SCOPE_AGENT);
}

}

extern "C" void kernel_launch(void* const* d_in, const int* in_sizes, int n_in, void* d_out, int out_size, void* d_ws,
                              size_t ws_size, hipStream_t stream) {
  (void)in_sizes; (void)n_in; (void)out_size; (void)ws_size;
  Params p;
  p.xA = (const char*)d_in[0]; p.kA = (const char*)d_in[1]; p.vA = (const char*)d_in[2]; p.wqA = (const char*)d_in[3]; p.woA = (const char*)d_in[4];
  p.xB = (const char*)d_in[5]; p.kB = (const char*)d_in[6]; p.vB = (const char*)d_in[7]; p.wqB = (const char*)d_in[8]; p.woB = (const char*)d_in[9];
  p.out = (char*)d_out; p.ws = (char*)d_ws;
  (void)hipFuncSetAttribute((const void*)fused_decode, hipFuncAttributeMaxDynamicSharedMemorySize, LDS_BYTES);
  hipLaunchKernelGGL(fused_decode, dim3(256), dim3(256), LDS_BYTES, stream, p);
}
```

```cpp
#include <hip/hip_runtime.h>
#include <stdint.h>
#define PACE 7
#define WE 6
#define WO 6
#define SA 780
#define TAU 226
#define SL1 3
#define SL2 0
#define SL3 4
#define TF4 2010
#define D4 5
#define SLP4 1
#define GAS __attribute__((address_space(1)))
#define LAS __attribute__((address_space(3)))
typedef unsigned v4u __attribute__((ext_vector_type(4)));
typedef unsigned v2u __attribute__((ext_vector_type(2)));
typedef float f32x4 __attribute__((ext_vector_type(4)));
typedef __bf16 bf16x8 __attribute__((ext_vector_type(8)));
typedef __bf16 bf16x2 __attribute__((ext_vector_type(2)));
typedef short s16x4 __attribute__((ext_vector_type(4)));
typedef GAS unsigned long long gu64;
typedef GAS unsigned gu32;

namespace {

constexpr int SK = 4096, DH = 128;
constexpr float SCL = 0.08838834764831845f * 1.4426950408889634f;
constexpr unsigned kSpinCap = 1u << 14;

constexpr int EPOCH_OFF = 0;
constexpr int T_OFF = 1024;
constexpr int Q_OFF = 4096, Q_BYTES = 2 * 8 * 2048 * 8;
constexpr int P_OFF = Q_OFF + Q_BYTES, P_BYTES = 2 * 8 * 4 * 8 * 4 * 64 * 8;
constexpr int L_OFF = P_OFF + P_BYTES, L_BYTES = 2 * 8 * 4 * 8 * 4 * 8;
constexpr int Y_OFF = L_OFF + L_BYTES, Y_BYTES = 2 * 32 * 8 * 32 * 16 * 8;
constexpr int WS_BYTES = Y_OFF + Y_BYTES;

constexpr int LDS_P1 = 0;
constexpr int LDS_Q = 8192;
constexpr int QROW = 272, QBR = 4 * QROW;
constexpr int LDS_ML = LDS_Q + 2 * QBR + 128;
constexpr int LDS_MRG = LDS_ML + 256;
constexpr int MROW = 132;
constexpr int LDS_WO = LDS_MRG + 2 * 4 * 4 * MROW * 4;
constexpr int LDS_KT = LDS_WO;
constexpr int LDS_OB = LDS_WO + 32768;
constexpr int LDS_RED = LDS_OB + 8 * 272;
constexpr int LDS_CNT = LDS_RED;
constexpr int LDS_END = LDS_CNT + 16;
constexpr int LDS_BYTES = 84 * 1024;
static_assert(LDS_END <= LDS_BYTES && LDS_KT % 256 == 0, "lds");
static_assert(LDS_ML % 16 == 0 && LDS_MRG % 16 == 0 && LDS_WO % 16 == 0 && LDS_OB % 16 == 0 && LDS_RED % 16 == 0, "align");

struct Params {
  const char* xA; const char* kA; const char* vA; const char* wqA; const char* woA;
  const char* xB; const char* kB; const char* vB; const char* wqB; const char* woB;
  char* out; char* ws;
};

template <typename T, typename F> __device__ __forceinline__ T bc(F x) { return __builtin_bit_cast(T, x); }
__device__ __forceinline__ f32x4 mfma(v4u a, v4u b, f32x4 c) {
  return __builtin_amdgcn_mfma_f32_16x16x32_bf16(bc<bf16x8>(a), bc<bf16x8>(b), c, 0, 0, 0);
}
__device__ __forceinline__ unsigned pk(float lo, float hi) { bf16x2 r = {(__bf16)lo, (__bf16)hi}; return bc<unsigned>(r); }
__device__ __forceinline__ float blo(unsigned u) { return bc<float>(u << 16); }
__device__ __forceinline__ float bhi(unsigned u) { return bc<float>(u & 0xffff0000u); }
__device__ __forceinline__ v4u ldnt(const GAS char* p) { return __builtin_nontemporal_load((const GAS v4u*)p); }
__device__ __forceinline__ v4u ldsc(__amdgpu_buffer_rsrc_t rs, int off) { return __builtin_amdgcn_raw_buffer_load_b128(rs, off, 0, 16); }
__device__ __forceinline__ void st_granule(char* ws, int off, unsigned val, unsigned tag) {
  __hip_atomic_store((gu64*)(ws + off), ((unsigned long long)tag << 32) | val, __ATOMIC_RELAXED, __HIP_MEMORY_SCOPE_AGENT);
}
__device__ __forceinline__ void st_buf8(__amdgpu_buffer_rsrc_t rs, int off, unsigned val, unsigned tag) {
  __builtin_amdgcn_raw_buffer_store_b64(v2u{val, tag}, rs, off, 0, 16);
}
__device__ __forceinline__ void st_buf16(__amdgpu_buffer_rsrc_t rs, int off, unsigned v0, unsigned v1, unsigned tag) {
  __builtin_amdgcn_raw_buffer_store_b128(v4u{v0, tag, v1, tag}, rs, off, 0, 16);
}
constexpr int OOB = 0x7ffffff0;
__device__ __forceinline__ unsigned now() { return (unsigned)__builtin_amdgcn_s_memrealtime(); }
__device__ __forceinline__ void lds_wait_count(unsigned addr, unsigned n) {
  unsigned t;
  asm volatile("1:\n\tds_read_b32 %0, %1\n\ts_waitcnt lgkmcnt(0)\n\tv_readfirstlane_b32 s90, %0\n\ts_cmp_lt_u32 s90, %2\n\t"
               "s_cbranch_scc0 2f\n\ts_sleep 1\n\ts_branch 1b\n2:" : "=&v"(t) : "v"(addr), "s"(n) : "s90", "scc", "memory");
}
__device__ __forceinline__ void win_wait(unsigned win) {
  asm volatile("s_cmp_gt_u32 %0, 8\n\ts_cbranch_scc1 1f\n\ts_waitcnt vmcnt(7)\n\t"
               "s_cmp_gt_u32 %0, 7\n\ts_cbranch_scc1 1f\n\ts_waitcnt vmcnt(6)\n\t"
               "s_cmp_gt_u32 %0, 6\n\ts_cbranch_scc1 1f\n\ts_waitcnt vmcnt(5)\n\t"
               "s_cmp_gt_u32 %0, 5\n\ts_cbranch_scc1 1f\n\ts_waitcnt vmcnt(4)\n\t"
               "s_cmp_gt_u32 %0, 4\n\ts_cbranch_scc1 1f\n\ts_waitcnt vmcnt(3)\n1:" :: "s"(win) : "scc", "memory");
}
#define L1 40
#define L2 100
#define L3 160
#define LM1 40
#define LM2 100000
__device__ __forceinline__ unsigned pick_win(unsigned sched, unsigned lo_) {
  unsigned wv;
  asm volatile("s_memrealtime s[90:91]\n\ts_waitcnt lgkmcnt(0)\n\ts_sub_u32 s90, s90, %1\n\ts_mov_b32 %0, %2\n\t"
               "s_cmp_gt_i32 s90, %7\n\ts_addc_u32 %0, %0, 0\n\t"
               "s_cmp_gt_i32 s90, %3\n\ts_addc_u32 %0, %0, 0\n\ts_cmp_gt_i32 s90, %4\n\ts_addc_u32 %0, %0, 0\n\t"
               "s_cmp_gt_i32 s90, %5\n\ts_addc_u32 %0, %0, 0\n\ts_cmp_gt_i32 s90, %6\n\ts_cselect_b32 %0, 16, %0"
               : "=&s"(wv) : "s"(sched), "s"(lo_), "i"(-LM1), "i"(L1), "i"(L2), "i"(L3), "i"(-LM2) : "s90", "s91", "scc");
  return wv;
}
#define SB() __builtin_amdgcn_sched_barrier(0)
#define WAITVM_(n) asm volatile("s_waitcnt vmcnt(" #n ")" ::: "memory")
#define WAITVM(n) WAITVM_(n)
#define RAW_BARRIER() asm volatile("s_waitcnt lgkmcnt(0)\n\ts_barrier" ::: "memory")

__device__ __forceinline__ v4u tcol(const v4u (&r)[8], int d) {
  const unsigned sel = (d & 1) ? 0x07060302u : 0x05040100u;
  const int c = d >> 1;
  v4u f;
  f.x = __builtin_amdgcn_perm(r[1][c], r[0][c], sel);
  f.y = __builtin_amdgcn_perm(r[3][c], r[2][c], sel);
  f.z = __builtin_amdgcn_perm(r[5][c], r[4][c], sel);
  f.w = __builtin_amdgcn_perm(r[7][c], r[6][c], sel);
  return f;
}
__device__ __forceinline__ float xrow_max(float x) {
  auto s = __builtin_amdgcn_permlane16_swap(bc<unsigned>(x), bc<unsigned>(x), false, false);
  x = fmaxf(bc<float>(s[0]), bc<float>(s[1]));
  auto t = __builtin_amdgcn_permlane32_swap(bc<unsigned>(x), bc<unsigned>(x), false, false);
  return fmaxf(bc<float>(t[0]), bc<float>(t[1]));
}
__device__ __forceinline__ float xrow_sum(float x) {
  auto s = __builtin_amdgcn_permlane16_swap(bc<unsigned>(x), bc<unsigned>(x), false, false);
  x = bc<float>(s[0]) + bc<float>(s[1]);
  auto t = __builtin_amdgcn_permlane32_swap(bc<unsigned>(x), bc<unsigned>(x), false, false);
  return bc<float>(t[0]) + bc<float>(t[1]);
}
template <int CTRL> __device__ __forceinline__ float dpp(float x) {
  return bc<float>(__builtin_amdgcn_mov_dpp(bc<int>(x), CTRL, 0xf, 0xf, true));
}
constexpr int DPP_ROR8 = 0x128;

typedef unsigned v16u __attribute__((ext_vector_type(16)));
#define POLL_LEFT "v_min3_u32 v252, %[o0], %[o1], %[o2]\n\t" \
  "v_min_u32 v252, v252, %[o3]\n\t" \
  "v_cmp_ne_u32_e64 s[80:81], v252, %[oob]\n\t" \
  "s_nop 1\n\t" \
  "s_cmp_eq_u64 s[80:81], 0\n\t" \
  "s_cbranch_scc1 2f\n\t"
#define POLL_ASK(a, b, c, d) \
  "buffer_load_dwordx4 v[" a "], %[o0], %[rs], 0 offen sc1\n\t" \
  "buffer_load_dwordx4 v[" b "], %[o1], %[rs], 0 offen sc1\n\t" \
  "buffer_load_dwordx4 v[" c "], %[o2], %[rs], 0 offen sc1\n\t" \
  "buffer_load_dwordx4 v[" d "], %[o3], %[rs], 0 offen sc1\n\t" \
  "s_sleep %[sl]\n\t"
#define POLL_TAGS(acc, t0, t1) \
  "v_xor_b32 " acc ", %[tag], v" t0 "\n\t" \
  "v_xor_b32 v252, %[tag], v" t1 "\n\t" \
  "v_or_b32 " acc ", " acc ", v252\n\t"
#define POLL_TAKE(i, m, x, z) \
  "v_cndmask_b32_e64 %[x" i "], %[x" i "], v" x ", " m "\n\t" \
  "v_cndmask_b32_e64 %[z" i "], %[z" i "], v" z ", " m "\n\t" \
  "v_cndmask_b32_e64 %[o" i "], %[o" i "], %[oob], " m "\n\t"
#define POLL_CHECK(x0, t0, z0, u0, x1, t1, z1, u1, x2, t2, z2, u2, x3, t3, z3, u3) \
  "s_waitcnt vmcnt(8)\n\t" \
  POLL_TAGS("v248", t0, u0) POLL_TAGS("v249", t1, u1) POLL_TAGS("v250", t2, u2) POLL_TAGS("v251", t3, u3) \
  "v_cmp_eq_u32_e64 s[80:81], 0, v248\n\t" \
  "v_cmp_eq_u32_e64 s[82:83], 0, v249\n\t" \
  "v_cmp_eq_u32_e64 s[84:85], 0, v250\n\t" \
  "v_cmp_eq_u32_e64 s[86:87], 0, v251\n\t" \
  "s_nop 1\n\t" \
  POLL_TAKE("0", "s[80:81]", x0, z0) POLL_TAKE("1", "s[82:83]", x1, z1) POLL_TAKE("2", "s[84:85]", x2, z2) POLL_TAKE("3", "s[86:87]", x3, z3) \
  POLL_LEFT
#define POLL_ASK_A POLL_ASK("200:203", "204:207", "208:211", "212:215")
#define POLL_ASK_B POLL_ASK("216:219", "220:223", "224:227", "228:231")
#define POLL_ASK_C POLL_ASK("232:235", "236:239", "240:243", "244:247")
#define POLL4_TEXT POLL_LEFT \
  "s_cmp_eq_u32 %[dly], 0\n\t" \
  "s_cbranch_scc1 3f\n\t" \
  "s_sleep %[dl]\n\t" \
  "3:\n\t" \
  POLL_ASK_A POLL_ASK_B POLL_ASK_C \
  "1:\n\t" \
  POLL_CHECK("200", "201", "202", "203", "204", "205", "206", "207", "208", "209", "210", "211", "212", "213", "214", "215") POLL_ASK_A \
  POLL_CHECK("216", "217", "218", "219", "220", "221", "222", "223", "224", "225", "226", "227", "228", "229", "230", "231") POLL_ASK_B \
  POLL_CHECK("232", "233", "234", "235", "236", "237", "238", "239", "240", "241", "242", "243", "244", "245", "246", "247") POLL_ASK_C \
  "s_sub_u32 %[cnt], %[cnt], 1\n\t" \
  "s_cbranch_scc0 1b\n\t" \
  "2:\n\t"
#define POLL_CLOB "v248", "v249", "v250", "v251", "v252", "v253", "s80", "s81", "s82", "s83", "s84", "s85", "s86", "s87", "s88", "s89", "scc", "memory"

__device__ __forceinline__ void ldL(__amdgpu_buffer_rsrc_t rs, int slot, v4u& la0, v4u& la1) {
  la0 = ldsc(rs, L_OFF + slot * 8);
  la1 = ldsc(rs, L_OFF + slot * 8 + 16);
}
struct LaneOff { unsigned v0, v1, kw, kr; };
__device__ __forceinline__ v4u ldV(const GAS char* t, const LaneOff& o, int j) { return ldnt(t + ((j < 4) ? o.v0 : o.v1) + (j & 3) * 1024); }

__device__ __forceinline__ void k_park(const v4u (&K)[8], LAS char* L, const LaneOff& o) {
#pragma unroll
  for (int j = 0; j < 8; ++j) *(LAS v4u*)(L + (o.kw ^ ((j & 3) << 4)) + j * 1024) = K[j];
  SB();
}
__device__ __forceinline__ void qk_part(LAS char* L, const LaneOff& o, const v4u (&qf)[4], f32x4& s0, f32x4& s1) {
  s0 = f32x4{0.f, 0.f, 0.f, 0.f}; s1 = s0;
#pragma unroll
  for (int st = 0; st < 4; ++st) {
    const LAS char* a = L + (o.kr ^ ((st & 1) << 6)) + (st >> 1) * 128;
    s0 = mfma(*(const LAS v4u*)a, qf[st], s0);
    s1 = mfma(*(const LAS v4u*)(a + 4096), qf[st], s1);
  }
  SB();
}
__device__ __forceinline__ void sv_part(const v4u (&V)[8], const f32x4& s0, const f32x4& s1, f32x4 (&acc)[8], float& m, float& l) {
  float t = fmaxf(fmaxf(fmaxf(s0[0], s0[1]), fmaxf(s0[2], s0[3])), fmaxf(fmaxf(s1[0], s1[1]), fmaxf(s1[2], s1[3])));
  t = xrow_max(t);
  const float mn = fmaxf(m, t);
  const float alpha = __builtin_amdgcn_exp2f((m - mn) * SCL);
  const float mc = mn * SCL;
  float p[8];
#pragma unroll
  for (int r = 0; r < 4; ++r) {
    p[r] = __builtin_amdgcn_exp2f(s0[r] * SCL - mc);
    p[4 + r] = __builtin_amdgcn_exp2f(s1[r] * SCL - mc);
  }
  l = l * alpha + (((p[0] + p[1]) + (p[2] + p[3])) + ((p[4] + p[5]) + (p[6] + p[7])));
  m = mn;
  const v4u P = {pk(p[0], p[1]), pk(p[2], p[3]), pk(p[4], p[5]), pk(p[6], p[7])};
#pragma unroll
  for (int d = 0; d < 8; ++d) {
    acc[d] *= alpha;
    acc[d] = mfma(tcol(V, d), P, acc[d]);
  }
  SB();
}

template <bool LD>
__device__ __forceinline__ void qk_park_ld(LAS char* L, const LaneOff& o, const v4u (&qf)[4], f32x4& s0, f32x4& s1, const v4u (&K)[8],
                                           v4u (&D)[8], const GAS char* t, unsigned win) {
  s0 = f32x4{0.f, 0.f, 0.f, 0.f}; s1 = s0;
#pragma unroll
  for (int st = 0; st < 4; ++st) {
    if (LD) { win_wait(win); D[st] = ldV(t, o, st); SB(); }
    const LAS char* a = L + (o.kr ^ ((st & 1) << 6)) + (st >> 1) * 128;
    s0 = mfma(*(const LAS v4u*)a, qf[st], s0);
    s1 = mfma(*(const LAS v4u*)(a + 4096), qf[st], s1);
    SB();
  }
#pragma unroll
  for (int j = 0; j < 8; ++j) {
    if (LD && (j & 1) == 0) { win_wait(win); D[4 + j / 2] = ldV(t, o, 4 + j / 2); SB(); }
    *(LAS v4u*)(L + (o.kw ^ ((j & 3) << 4)) + j * 1024) = K[j];
    if (LD && (j & 1)) SB();
  }
  SB();
}
template <bool LD>
__device__ __forceinline__ void sv_ld(const v4u (&V)[8], const f32x4& s0, const f32x4& s1, f32x4 (&acc)[8], float& m, float& l,
                                      v4u (&D)[8], const GAS char* t, const LaneOff& o, unsigned win) {
  if (LD) { win_wait(win); D[0] = ldV(t, o, 0); SB(); }
  float tm = fmaxf(fmaxf(fmaxf(s0[0], s0[1]), fmaxf(s0[2], s0[3])), fmaxf(fmaxf(s1[0], s1[1]), fmaxf(s1[2], s1[3])));
  tm = xrow_max(tm);
  const float mn = fmaxf(m, tm);
  const float alpha = __builtin_amdgcn_exp2f((m - mn) * SCL);
  const float mc = mn * SCL;
  if (LD) { SB(); win_wait(win); D[1] = ldV(t, o, 1); SB(); }
  float p[8];
#pragma unroll
  for (int r = 0; r < 4; ++r) {
    p[r] = __builtin_amdgcn_exp2f(s0[r] * SCL - mc);
    p[4 + r] = __builtin_amdgcn_exp2f(s1[r] * SCL - mc);
  }
  l = l * alpha + (((p[0] + p[1]) + (p[2] + p[3])) + ((p[4] + p[5]) + (p[6] + p[7])));
  m = mn;
  const v4u P = {pk(p[0], p[1]), pk(p[2], p[3]), pk(p[4], p[5]), pk(p[6], p[7])};
#pragma unroll
  for (int d = 0; d < 8; ++d) {
    if (LD && d < 6) { SB(); win_wait(win); D[2 + d] = ldV(t, o, 2 + d); SB(); }
    acc[d] *= alpha;
    acc[d] = mfma(tcol(V, d), P, acc[d]);
  }
  SB();
}

__device__ __forceinline__ void park_partial(LAS char* L, int br, int w, int lane, const f32x4 (&acc)[8], float m, float l) {
  const float ls = xrow_sum(l);
  const int n = lane & 15, g = lane >> 4;
  if (n < 4) {
    LAS float* o = (LAS float*)(L + LDS_MRG) + ((br * 4 + w) * 4 + n) * MROW + g * 32;
#pragma unroll
    for (int d = 0; d < 8; ++d) *(LAS f32x4*)(o + d * 4) = acc[d];
    if (g == 0) { LAS float* ml = (LAS float*)(L + LDS_ML) + ((br * 4 + w) * 4 + n) * 2; ml[0] = m; ml[1] = ls; }
  }
}

__device__ __forceinline__ void merge(LAS char* L, int br, int h, int dp, unsigned& val, unsigned& lse) {
  const int D = 2 * dp, idx = (D >> 5) * 32 + (D & 7) * 4 + ((D >> 3) & 3);
  const LAS float* ml = (const LAS float*)(L + LDS_ML) + (br * 16 + h) * 2;
  float mw[4], lw[4], a0[4], a1[4];
#pragma unroll
  for (int u = 0; u < 4; ++u) {
    const LAS float* o = (const LAS float*)(L + LDS_MRG) + ((br * 4 + u) * 4 + h) * MROW + idx;
    mw[u] = ml[u * 8]; lw[u] = ml[u * 8 + 1]; a0[u] = o[0]; a1[u] = o[4];
  }
  const float M = fmaxf(fmaxf(mw[0], mw[1]), fmaxf(mw[2], mw[3]));
  float Ls = 0.f, o0 = 0.f, o1 = 0.f;
#pragma unroll
  for (int u = 0; u < 4; ++u) {
    const float f = __builtin_amdgcn_exp2f((mw[u] - M) * SCL);
    Ls += f * lw[u]; o0 += f * a0[u]; o1 += f * a1[u];
  }
  const float inv = __builtin_amdgcn_rcpf(Ls);
  val = pk(o0 * inv, o1 * inv);
  lse = bc<unsigned>(M * SCL + __builtin_amdgcn_logf(Ls));
}
__device__ __forceinline__ void publish(__amdgpu_buffer_rsrc_t rs, int dp, int slot, unsigned val, unsigned lse, unsigned tag) {
  const unsigned nb = (unsigned)__builtin_amdgcn_mov_dpp((int)val, 0xb1, 0xf, 0xf, true);
  st_buf16(rs, (dp & 1) ? OOB : P_OFF + (slot * 64 + dp) * 8, val, nb, tag);
  st_buf8(rs, dp == 0 ? L_OFF + slot * 8 : OOB, lse, tag);
}

__global__ void __launch_bounds__(256, 2) fused_decode(Params p) {
  extern __shared__ __attribute__((aligned(16))) char smem[];
  LAS char* L = (LAS char*)smem;
  const int tid = threadIdx.x, lane = tid & 63, w = __builtin_amdgcn_readfirstlane(tid >> 6);
  const int wg = blockIdx.x, kvh = wg & 7, jj = wg >> 3;
  const __amdgpu_buffer_rsrc_t rs = __builtin_amdgcn_make_buffer_rsrc(p.ws, 0, WS_BYTES, 0x00020000);

  const unsigned t_entry = now();
  unsigned long long pc_; int clen_;
  asm volatile("s_getpc_b64 %0\n.Lfd_pc0:\n\ts_mov_b32 %1, .Lfd_end0-.Lfd_pc0" : "=s"(pc_), "=s"(clen_));
  unsigned pf_[2];
#pragma unroll
  for (int i = 0; i < 2; ++i)
    pf_[i] = *(const GAS unsigned*)((const GAS char*)pc_ + (unsigned)max(min((jj < 1 ? tid : 0) * 128 + i * 64, clen_ - 4) & ~3, 0));
  const unsigned tag = max(__hip_atomic_load((gu32*)(p.ws + EPOCH_OFF), __ATOMIC_RELAXED, __HIP_MEMORY_SCOPE_AGENT) + 1u, 1u);
  const int br1 = jj >> 4;
  const int kv1 = kvh, cg = jj & 15;
  const int s2 = kvh >> 1, rj = (jj - 3 * s2) & 31, pr2 = (rj & 7) | ((kvh & 1) << 3) | ((rj >> 3) << 4), b2 = pr2 >> 3, kv2 = pr2 & 7;
  const size_t pair = (size_t)(b2 * 8 + kv2) * SK * 256;
  LaneOff lo;
  {
    const int i = lane & 15, g = lane >> 4;
    lo.v0 = (unsigned)lane * 16u;
    lo.v1 = lo.v0 + 4096u;
    lo.kw = (unsigned)(LDS_KT + w * 8192 + g * 256 + ((i ^ ((g & 1) << 2)) << 4));
    lo.kr = (unsigned)(LDS_KT + w * 8192 + (4 * (i & 3) + (i >> 2)) * 256 + ((g ^ (i & 7)) << 4));
  }
  const int g16 = s2 * 4 + w;
  constexpr int TB = 32 * 256;
  const GAS char* kA = (const GAS char*)p.kA + pair + (size_t)(3072 * 256 + g16 * TB);
  const GAS char* vA = (const GAS char*)p.vA + pair + (size_t)(3072 * 256 + g16 * TB);
  const GAS char* kB = (const GAS char*)p.kB + pair + (size_t)(g16 * TB);
  const GAS char* vB = (const GAS char*)p.vB + pair + (size_t)(g16 * TB);

  v4u xa[2], wv[8];
  v4u K0[8], V0[8], K1[8], V1[8];
  {
    const GAS char* x = (const GAS char*)(br1 ? p.xB : p.xA);
    const int m = lane & 15, g = lane >> 4, h1 = m >> 3, b = m & 7;
#pragma unroll
    for (int ps = 0; ps < 2; ++ps) xa[ps] = *(const GAS v4u*)(x + (b * 512 + 128 * w + 8 * (4 * g + 2 * ps + h1)) * 2);
    const GAS char* wq = (const GAS char*)(br1 ? p.wqB : p.wqA) + (size_t)(128 * w) * 8192 + (kv1 * 512 + 32 * cg) * 2;
    const unsigned wo_ = (unsigned)(8 * (lane >> 2)) * 8192u + (lane & 3) * 16u;
#pragma unroll
    for (int j = 0; j < 8; ++j) {
      const GAS char* wj = wq + j * 8192u;
      asm("" : "+s"(wj));
      wv[j] = ldnt(wj + wo_);
    }
    SB();
    WAITVM(7);
#pragma unroll
    for (int j = 0; j < 8; ++j) K0[j] = ldV(kA, lo, j);
  }
  SB();

  {
    f32x4 a0[8], a1[8];
#pragma unroll
    for (int d = 0; d < 8; ++d) {
      const v4u f = tcol(wv, d);
      const f32x4 z = {0.f, 0.f, 0.f, 0.f};
      a0[d] = mfma(xa[0], f, z);
      a1[d] = mfma(xa[1], f, z);
    }
    const int n = lane & 15, g = lane >> 4, kk = n >> 2, c2 = n & 3, h1 = g >> 1;
    const bool hi = (kk >> 1) != 0;
    LAS float* dst = (LAS float*)(L + LDS_P1) + ((w * 2 + h1) * 2 + (g & 1)) * 128 + c2 * 32;
#pragma unroll
    for (int d = 0; d < 8; ++d) {
      f32x4 v;
#pragma unroll
      for (int r = 0; r < 4; ++r) { const float t = hi ? a1[d][r] : a0[d][r]; v[r] = t + dpp<DPP_ROR8>(t); }
      if (kk == h1) *(LAS f32x4*)(dst + d * 4) = v;
    }
  }

  if (tid == 0) *(LAS unsigned*)(L + LDS_CNT) = 0u;
  RAW_BARRIER();
  if (wg < 8 && tid == 128) st_granule(p.ws, T_OFF + wg * 8, t_entry, tag);
  if (tid < 128) {
    const int b = tid >> 4, pr = tid & 15;
    float q0 = 0.f, q1 = 0.f;
    const int c0 = 2 * pr;
    const LAS float* src = (const LAS float*)(L + LDS_P1) + (b >> 2) * 128 + (c0 >> 3) * 32 + (c0 & 7) * 4 + (b & 3);
#pragma unroll
    for (int u = 0; u < 8; ++u) { q0 += src[u * 256]; q1 += src[u * 256 + 4]; }
    st_granule(p.ws, Q_OFF + ((br1 * 8 + b) * 2048 + kv1 * 256 + cg * 16 + pr) * 8, pk(q0, q1), tag);
  }

  SB();

#pragma unroll
  for (int j = 0; j < 8; ++j) { WAITVM(PACE); V0[j] = ldV(vA, lo, j); SB(); }
#pragma unroll
  for (int j = 0; j < 8; ++j) { WAITVM(PACE); K1[j] = ldV(kA + 16 * TB, lo, j); SB(); }
#pragma unroll
  for (int j = 0; j < 8; ++j) { WAITVM(PACE); V1[j] = ldV(vA + 16 * TB, lo, j); SB(); }
  k_park(K0, L, lo);
  asm volatile("" :: "v"(pf_[0]));
  asm volatile("" :: "v"(pf_[1]));
  const int qoff = Q_OFF + (((w >> 1) * 8 + b2) * 2048 + kv2 * 256 + (w & 1) * 128 + 2 * lane) * 8;
  const int toff = T_OFF + (lane & 3) * 16;
  v4u qg, tg;
  WAITVM(PACE);
  qg = ldsc(rs, qoff);
  tg = ldsc(rs, toff);
  SB();
#pragma unroll
  for (int j = 0; j < 8; ++j) { WAITVM(PACE); K0[j] = ldV(kB, lo, j); SB(); }

  unsigned S0;
  {
    if (!__all(qg.y == tag && qg.w == tag && tg.y == tag && tg.w == tag)) {
      unsigned spins = 0;
      do {
        __builtin_amdgcn_s_sleep(2);
        asm volatile("" ::: "memory");
        qg = ldsc(rs, qoff);
        tg = ldsc(rs, toff);
      } while (!__all(qg.y == tag && qg.w == tag && tg.y == tag && tg.w == tag) && ++spins < kSpinCap);
    }
    const int col = (w & 1) * 256 + 4 * lane;
    *(LAS v2u*)(L + LDS_Q + (w >> 1) * QBR + (col >> 7) * QROW + (col & 127) * 2) = v2u{qg.x, qg.z};
    int d = min((int)(tg.x - t_entry), (int)(tg.z - t_entry));
    d = min(d, __builtin_amdgcn_mov_dpp(d, 0xb1, 0xf, 0xf, true));
    d = min(d, __builtin_amdgcn_mov_dpp(d, 0x4e, 0xf, 0xf, true));
    S0 = t_entry + (unsigned)__builtin_amdgcn_readfirstlane(d) + SA;
  }

  RAW_BARRIER();

  v4u qf[4];
  f32x4 acc[8];
  float m, l;
  const int qlane = (lane & 3) * QROW + (lane >> 4) * 16;
#pragma unroll
  for (int st = 0; st < 4; ++st) qf[st] = *(const LAS v4u*)(L + LDS_Q + qlane + st * 64);
#pragma unroll
  for (int d = 0; d < 8; ++d) acc[d] = f32x4{0.f, 0.f, 0.f, 0.f};
  m = -INFINITY; l = 0.f;
  f32x4 s0, s1;
  const unsigned wbase = (kvh & 1) ? WO : WE;
  unsigned win = wbase;
#define PICK(t) win = pick_win(S0 + (t) * TAU - ((t) ? TAU / 2 : 0), wbase - 2u)
#define STEPI(t, R, nk, V, Vp, nv) do { PICK(t); qk_park_ld<true>(L, lo, qf, s0, s1, R, Vp, nv, win); sv_ld<true>(V, s0, s1, acc, m, l, R, nk, lo, win); } while (0)
  PICK(0); qk_park_ld<false>(L, lo, qf, s0, s1, K1, V1, vB, win); sv_ld<true>(V0, s0, s1, acc, m, l, K1, kB + 16 * TB, lo, win);
  STEPI(1, K0, kB + 32 * TB, V1, V0, vB);
  park_partial(L, 0, w, lane, acc, m, l);
  if (lane == 0) __hip_atomic_fetch_add((LAS unsigned*)(L + LDS_CNT), 1u, __ATOMIC_RELAXED, __HIP_MEMORY_SCOPE_WORKGROUP);

#pragma unroll
  for (int st = 0; st < 4; ++st) qf[st] = *(const LAS v4u*)(L + LDS_Q + QBR + qlane + st * 64);
#pragma unroll
  for (int d = 0; d < 8; ++d) acc[d] = f32x4{0.f, 0.f, 0.f, 0.f};
  m = -INFINITY; l = 0.f;
  unsigned aval = 0, alse = 0;
  STEPI(2, K1, kB + 48 * TB, V0, V1, vB + 16 * TB);
  STEPI(3, K0, kB + 64 * TB, V1, V0, vB + 32 * TB);
  lds_wait_count((unsigned)(uintptr_t)(L + LDS_CNT), 4u);
  merge(L, 0, w, lane, aval, alse);
  SB();
  STEPI(4, K1, kB + 80 * TB, V0, V1, vB + 48 * TB);
  STEPI(5, K0, kB + 96 * TB, V1, V0, vB + 64 * TB);
  STEPI(6, K1, kB + 112 * TB, V0, V1, vB + 80 * TB);
  PICK(7);
  qk_park_ld<true>(L, lo, qf, s0, s1, K0, V0, vB + 96 * TB, win); sv_ld<false>(V1, s0, s1, acc, m, l, K0, kB, lo, win);
  PICK(8);
  qk_park_ld<true>(L, lo, qf, s0, s1, K1, V1, vB + 112 * TB, win);

  publish(rs, lane, (kv2 * 4 + w) * 32 + b2 * 4 + s2, aval, alse, tag);
  SB();
  int tid_ = tid;
  asm volatile("" : "+v"(tid_));
  {
  const int tid = tid_, lane = tid & 63;
  const int hh = jj >> 3, br3 = (jj >> 2) & 1, qt = jj & 3;
  sv_part(V0, s0, s1, acc, m, l);
  qk_part(L, lo, qf, s0, s1);
  {
    const GAS char* wb = (const GAS char*)(br3 ? p.woB : p.woA) + (size_t)((kvh * 4 + hh) * 128 + 32 * w) * 1024 + qt * 256;
    const unsigned wl = (unsigned)(lane >> 4) * 1024u + (lane & 15) * 16u;
#pragma unroll
    for (int i = 0; i < 8; ++i) K0[i] = ldnt(wb + wl + i * 4096);
    SB();
  }
  sv_part(V1, s0, s1, acc, m, l);

  park_partial(L, 1, w, lane, acc, m, l);

  RAW_BARRIER();

  const int slot3 = ((br3 * 8 + kvh) * 4 + hh) * 32 + (tid >> 5) * 4;
  v4u pa[4], la0, la1;
  {
    unsigned bval, blse;
    merge(L, 1, w, lane, bval, blse);
    publish(rs, lane, ((8 + kv2) * 4 + w) * 32 + b2 * 4 + s2, bval, blse, tag);
  }

  SB();
  {
    LAS char* wd = L + LDS_WO + (32 * w + (lane >> 4)) * 256 + ((lane & 1) << 4);
    const int c = ((lane & 15) >> 1) ^ (lane >> 4);
#pragma unroll
    for (int i = 0; i < 8; ++i) *(LAS v4u*)(wd + i * 1024 + ((c ^ (((i >> 1) & 1) << 2)) << 5)) = K0[i];
  }
  RAW_BARRIER();
  v4u wf[2][4];
  {
    const int n = lane & 15, g = lane >> 4, swz = (n >> 2) | ((g & 1) << 2);
#pragma unroll
    for (int st = 0; st < 4; ++st) {
      const int row = 32 * st + 8 * g + (n >> 2);
#pragma unroll
      for (int t = 0; t < 2; ++t) {
        const LAS char* wp = L + LDS_WO + row * 256 + (((2 * w + t) ^ swz) << 5) + (n & 3) * 8;
        const v2u u0 = bc<v2u>(__builtin_amdgcn_ds_read_tr16_b64_v4i16((LAS s16x4*)wp));
        const v2u u1 = bc<v2u>(__builtin_amdgcn_ds_read_tr16_b64_v4i16((LAS s16x4*)(wp + 4 * 256)));
        wf[t][st] = v4u{u0.x, u0.y, u1.x, u1.y};
      }
    }
  }

  {
    const int b = tid >> 5, dq = tid & 31, slot = slot3;
    unsigned spins = 0;
    bool ok[4] = {false, false, false, false};
    bool lok;
    {
      for (;;) {
        asm volatile("" ::: "memory");
        ldL(rs, slot, la0, la1);
        const int nv = (la0.y == tag) + (la0.w == tag) + (la1.y == tag) + (la1.w == tag);
        if (__all(nv >= 3) || ++spins > kSpinCap) break;
        __builtin_amdgcn_s_sleep(SL1);
      }

      lok = la0.y == tag && la0.w == tag && la1.y == tag && la1.w == tag;
      for (;;) {
        asm volatile("" ::: "memory");
#pragma unroll
        for (int s = 0; s < 4; ++s) if (!ok[s]) pa[s] = ldsc(rs, P_OFF + ((slot + s) * 64 + 2 * dq) * 8);
        if (!lok) ldL(rs, slot, la0, la1);
#pragma unroll
        for (int s = 0; s < 4; ++s) ok[s] = pa[s].y == tag && pa[s].w == tag;
        lok = la0.y == tag && la0.w == tag && la1.y == tag && la1.w == tag;

        if (__all(ok[0] && ok[1] && ok[2] && ok[3] && lok) || ++spins > kSpinCap) break;
        __builtin_amdgcn_s_sleep(SL2);
      }
    }

    const float l0 = bc<float>(la0.x), l1 = bc<float>(la0.z), l2 = bc<float>(la1.x), l3 = bc<float>(la1.z);
    const float M = fmaxf(fmaxf(l0, l1), fmaxf(l2, l3));
    const float ws_[4] = {__builtin_amdgcn_exp2f(l0 - M), __builtin_amdgcn_exp2f(l1 - M), __builtin_amdgcn_exp2f(l2 - M), __builtin_amdgcn_exp2f(l3 - M)};
    const float inv = __builtin_amdgcn_rcpf((ws_[0] + ws_[1]) + (ws_[2] + ws_[3]));
    float o[4] = {0.f, 0.f, 0.f, 0.f};
#pragma unroll
    for (int s = 0; s < 4; ++s) {
      o[0] += ws_[s] * blo(pa[s].x); o[1] += ws_[s] * bhi(pa[s].x);
      o[2] += ws_[s] * blo(pa[s].z); o[3] += ws_[s] * bhi(pa[s].z);
    }
    *(LAS v2u*)(L + LDS_OB + b * 272 + dq * 8) = v2u{pk(o[0] * inv, o[1] * inv), pk(o[2] * inv, o[3] * inv)};
  }
  RAW_BARRIER();
  {
    const int n = lane & 15, g = lane >> 4;
    f32x4 y[2] = {f32x4{0.f, 0.f, 0.f, 0.f}, f32x4{0.f, 0.f, 0.f, 0.f}};
#pragma unroll
    for (int st = 0; st < 4; ++st) {
      const v4u a = *(const LAS v4u*)(L + LDS_OB + (n & 7) * 272 + st * 64 + g * 16);
      y[0] = mfma(a, wf[0][st], y[0]);
      y[1] = mfma(a, wf[1][st], y[1]);
    }
    const f32x4 yy = (g >> 1) ? y[1] : y[0];
    const bool odd = n & 1;
    const float s0_ = odd ? yy[0] : yy[2], s1_ = odd ? yy[1] : yy[3];
    const float n0 = dpp<0xb1>(s0_), n1 = dpp<0xb1>(s1_);
    const float a0 = odd ? n0 : yy[0], b0 = odd ? yy[2] : n0;
    const float a1 = odd ? n1 : yy[1], b1 = odd ? yy[3] : n1;
    const int r0 = 4 * (g & 1) + (odd ? 2 : 0);
    const int yo = Y_OFF + ((((br3 * 32 + qt * 8 + 2 * w + (g >> 1)) * 8 + r0) * 32 + kvh * 4 + hh) * 16 + (n & ~1)) * 8;
    st_buf16(rs, yo, bc<unsigned>(a0), bc<unsigned>(b0), tag);
    st_buf16(rs, yo + 32 * 16 * 8, bc<unsigned>(a1), bc<unsigned>(b1), tag);
  }

  if (w < 2) {
    const int br = 1 - w, np = lane & 7, pl = lane >> 3;
    const int yoff = Y_OFF + ((((br * 32 + jj) * 8 + kvh) * 32 + pl) * 16 + 2 * np) * 8;
    v4u ya[4];
    bool ok[4] = {false, false, false, false};
    unsigned spins = 0;
    unsigned dly = 1;
#pragma unroll
    for (int i = 0; i < 4; ++i) ya[i] = v4u{0u, 0u, 0u, 0u};
    while ((int)(now() - (S0 + TF4)) < 0 && ++spins <= kSpinCap) {
      dly = 0;
      asm volatile("" ::: "memory");
#pragma unroll
      for (int i = 0; i < 4; ++i) if (!ok[i]) ya[i] = ldsc(rs, yoff + i * 8 * 128);
#pragma unroll
      for (int i = 0; i < 4; ++i) ok[i] = ya[i].y == tag && ya[i].w == tag;
      if (__all(ok[0] && ok[1] && ok[2] && ok[3])) break;
      __builtin_amdgcn_s_sleep(SL3);
    }
    v16u YA, YB, YC;
    {
      unsigned o0 = ok[0] ? OOB : yoff, o1 = ok[1] ? OOB : yoff + 1024, o2 = ok[2] ? OOB : yoff + 2048, o3 = ok[3] ? OOB : yoff + 3072;
      unsigned x0 = ya[0].x, x1 = ya[1].x, x2 = ya[2].x, x3 = ya[3].x, z0 = ya[0].z, z1 = ya[1].z, z2 = ya[2].z, z3 = ya[3].z, cnt = kSpinCap;
      dly = __builtin_amdgcn_readfirstlane(dly);
      asm volatile(POLL4_TEXT
                   : [a0] "=&{v[200:215]}"(YA), [b0] "=&{v[216:231]}"(YB), [c0] "=&{v[232:247]}"(YC),
                     [x0] "+v"(x0), [x1] "+v"(x1), [x2] "+v"(x2), [x3] "+v"(x3), [z0] "+v"(z0), [z1] "+v"(z1), [z2] "+v"(z2), [z3] "+v"(z3),
                     [o0] "+v"(o0), [o1] "+v"(o1), [o2] "+v"(o2), [o3] "+v"(o3), [cnt] "+s"(cnt)
                   : [rs] "s"(rs), [tag] "s"(tag), [oob] "v"((unsigned)OOB), [sl] "i"(SLP4), [dly] "s"(dly), [dl] "i"(D4)
                   : POLL_CLOB);
      ya[0].x = x0; ya[1].x = x1; ya[2].x = x2; ya[3].x = x3; ya[0].z = z0; ya[1].z = z1; ya[2].z = z2; ya[3].z = z3;
    }

    float v0 = (bc<float>(ya[0].x) + bc<float>(ya[1].x)) + (bc<float>(ya[2].x) + bc<float>(ya[3].x));
    float v1 = (bc<float>(ya[0].z) + bc<float>(ya[1].z)) + (bc<float>(ya[2].z) + bc<float>(ya[3].z));
    v0 += dpp<DPP_ROR8>(v0); v1 += dpp<DPP_ROR8>(v1);
    v0 = xrow_sum(v0); v1 = xrow_sum(v1);
    if (lane < 8) *(GAS unsigned*)((GAS char*)p.out + (kvh * 1024 + br * 512 + jj * 16 + 2 * np) * 2) = pk(v0, v1);
    asm volatile("s_waitcnt vmcnt(0)" :: "{v[200:215]}"(YA), "{v[216:231]}"(YB), "{v[232:247]}"(YC) : "memory");
  }
  }
  asm volatile(".Lfd_end0:");

  if (wg == 0 && tid == 0) __hip_atomic_store((gu32*)(p.ws + EPOCH_OFF), tag, __ATOMIC_RELAXED, __HIP_MEMORY_SCOPE_AGENT);
}

}

extern "C" void kernel_launch(void* const* d_in, const int* in_sizes, int n_in, void* d_out, int out_size, void* d_ws,
                              size_t ws_size, hipStream_t stream) {
  (void)in_sizes; (void)n_in; (void)out_size; (void)ws_size;
  Params p;
  p.xA = (const char*)d_in[0]; p.kA = (const char*)d_in[1]; p.vA = (const char*)d_in[2]; p.wqA = (const char*)d_in[3]; p.woA = (const char*)d_in[4];
  p.xB = (const char*)d_in[5]; p.kB = (const char*)d_in[6]; p.vB = (const char*)d_in[7]; p.wqB = (const char*)d_in[8]; p.woB = (const char*)d_in[9];
  p.out = (char*)d_out; p.ws = (char*)d_ws;
  (void)hipFuncSetAttribute((const void*)fused_decode, hipFuncAttributeMaxDynamicSharedMemorySize, LDS_BYTES);
  hipLaunchKernelGGL(fused_decode, dim3(256), dim3(256), LDS_BYTES, stream, p);
}
```
